```python
import torch

def _softmax(x, axis=-1):
    return torch.softmax(x, dim=axis)

def _gelu(x):
    return torch.nn.functional.gelu(x, approximate='tanh')

def _silu(x):
    return torch.nn.functional.silu(x)

def _sigmoid(x):
    return torch.sigmoid(x)

def _layernorm(x, axis=-1, eps=1e-05):
    m = x.mean(dim=axis, keepdim=True)
    v = x.var(dim=axis, keepdim=True, unbiased=False)
    return (x - m) / torch.sqrt(v + eps)

def _scatter_add(tgt, idx, src, axis=0):
    return tgt.clone().index_add_(axis, idx, src)

def _segment_sum(src, seg, n):
    out = torch.zeros((n,) + src.shape[1:], dtype=src.dtype)
    return out.index_add_(0, seg, src)

def _pad(x, pw):
    flat = tuple((v for pair in reversed(pw) for v in pair))
    return torch.nn.functional.pad(x, flat)
kernel_name = 'parallel_sum__memory_aug_recurrent_mem_tr__moe_arch_mod__prompt'

def setup_inputs():

    _w = lambda a: (lambda t: t.to(torch.bfloat16) if t.is_floating_point() else t)(torch.as_tensor(a))

    def _A_setup():
        B, S, Dm = (1, 2048, 512)
        H, Dh = (32, 128)
        M, seg = (8, 32)
        seg = min(seg, S)
        S = S // seg * seg
        return {'x': _w((lambda *s: torch.randn(*s, dtype=torch.bfloat16) * 0.05)(B, S, Dm)), 'mem0': _w((lambda *s: torch.randn(*s, dtype=torch.bfloat16) * 1.6)(B, M, Dm)), 'Wq': _w((lambda *s: torch.randn(*s, dtype=torch.bfloat16) * 0.8)(Dm, H, Dh) * Dm ** (-0.5)), 'Wk': _w((lambda *s: torch.randn(*s, dtype=torch.bfloat16) * 0.8)(Dm, H, Dh) * Dm ** (-0.5)), 'Wv': _w((lambda *s: torch.randn(*s, dtype=torch.bfloat16) * 1.6)(Dm, H, Dh) * Dm ** (-0.5)), 'Wo': _w((lambda *s: torch.randn(*s, dtype=torch.bfloat16) * 1.6)(H, Dh, Dm) * (H * Dh) ** (-0.5)), 'seg_len': seg}
    _A_in = _A_setup()

    def _B_setup():
        B, S, D, F, C = (1, 2048, 512, 1792, 2)
        return {'x': _w((lambda *s: torch.randn(*s, dtype=torch.bfloat16) * 0.05)(B, S, D)), 'w_r': _w((lambda *s: torch.randn(*s, dtype=torch.bfloat16) * 0.05)(D)), 'w_gate': _w((lambda *s: torch.randn(*s, dtype=torch.bfloat16) * 0.05)(D, F)), 'w_up': _w((lambda *s: torch.randn(*s, dtype=torch.bfloat16) * 0.05)(D, F)), 'w_down': _w((lambda *s: torch.randn(*s, dtype=torch.bfloat16) * 0.05)(F, D))}
    _B_in = _B_setup()
    return {'A_x': _A_in['x'], 'A_mem0': _A_in['mem0'], 'A_Wq': _A_in['Wq'], 'A_Wk': _A_in['Wk'], 'A_Wv': _A_in['Wv'], 'A_Wo': _A_in['Wo'], 'A_seg_len': _A_in['seg_len'], 'B_x': _B_in['x'], 'B_w_r': _B_in['w_r'], 'B_w_gate': _B_in['w_gate'], 'B_w_up': _B_in['w_up'], 'B_w_down': _B_in['w_down']}

def reference(A_x, A_mem0, A_Wq, A_Wk, A_Wv, A_Wo, A_seg_len, B_x, B_w_r, B_w_gate, B_w_up, B_w_down):

    def _A_ref(x, mem0, Wq, Wk, Wv, Wo, seg_len):
        B, S, Dm = x.shape
        H, Dh = (Wq.shape[1], Wq.shape[2])
        M = mem0.shape[1]
        scale = Dh ** (-0.5)
        n_seg = S // seg_len
        L = M + seg_len
        idx = torch.arange(L)
        mask = (idx[:, None] < M) | (idx[:, None] >= idx[None, :])
        mem = mem0
        outs = []
        for s in range(n_seg):
            xs = x[:, s * seg_len:(s + 1) * seg_len, :].clone()
            h = torch.concatenate([mem, xs], axis=1)
            q = torch.einsum('btd,dhe->bhte', h, Wq)
            k = torch.einsum('btd,dhe->bhte', h, Wk)
            v = torch.einsum('btd,dhe->bhte', h, Wv)
            scores = torch.einsum('bhqe,bhke->bhqk', q, k) * scale
            scores = torch.where(mask[None, None], scores, -1000000000.0)
            p = _softmax(scores, axis=-1)
            o = torch.einsum('bhqk,bhke->bhqe', p, v)
            o = torch.einsum('bhte,hed->btd', o, Wo)
            mem = o[:, :M, :].clone()
            outs.append(o[:, M:, :])
        return torch.concatenate(outs, axis=1)

    def _B_ref(x, w_r, w_gate, w_up, w_down):
        B, S, D = x.shape
        C = 2
        r = x @ w_r
        idx = torch.argsort(-r, axis=-1)[:, :C]
        idx_e = (lambda x, n, axis: torch.repeat_interleave(x, n, dim=axis))(idx.reshape(B, C, 1), D, axis=2)
        x_sel = (lambda x, i, axis: torch.take_along_dim(x, i, dim=axis))(x, idx_e, axis=1)
        g = _silu(x_sel @ w_gate)
        u = x_sel @ w_up
        y = g * u @ w_down
        r_sel = _sigmoid((lambda x, i, axis: torch.take_along_dim(x, i, dim=axis))(r, idx, axis=1))
        y = y * r_sel.reshape(B, C, 1)
        b_off = (torch.arange(B) * S).reshape(B, 1)
        flat_idx = (idx + b_off).reshape(B * C)
        zero = (x * 0.0).reshape(B * S, D)
        upd = _scatter_add(zero, flat_idx, y.reshape(B * C, D), axis=0)
        return x + upd.reshape(B, S, D)
    x = A_x
    xn = _layernorm(x, axis=-1)
    return x + _A_ref(xn, A_mem0, A_Wq, A_Wk, A_Wv, A_Wo, A_seg_len) + _B_ref(xn, B_w_r, B_w_gate, B_w_up, B_w_down)
```

```cpp
#include <hip/hip_runtime.h>
#include <hip/hip_bf16.h>
#include <stdint.h>

#define GAS __attribute__((address_space(1)))
#define LAS __attribute__((address_space(3)))
typedef unsigned short u16;
typedef unsigned char u8;
typedef unsigned v4u __attribute__((ext_vector_type(4)));
typedef unsigned v2u __attribute__((ext_vector_type(2)));
typedef unsigned v8u __attribute__((ext_vector_type(8)));
typedef float f32x4 __attribute__((ext_vector_type(4)));
typedef __bf16 bf16x2 __attribute__((ext_vector_type(2)));
typedef __bf16 bf16x8 __attribute__((ext_vector_type(8)));
typedef __bf16 bf16x16 __attribute__((ext_vector_type(16)));
typedef short s16x4 __attribute__((ext_vector_type(4)));
typedef GAS unsigned gu32;
#define RLX_AGENT __ATOMIC_RELAXED, __HIP_MEMORY_SCOPE_AGENT

namespace {

constexpr int S_ = 2048, D_ = 512, H_ = 32, DH_ = 128, NSEG = 64, F_ = 1792;
constexpr size_t MB = 1u << 20;
constexpr size_t OFF_PART = 0;
constexpr size_t OFF_M = 2 * MB;
constexpr size_t OFF_CTL = 3 * MB;
constexpr size_t OFF_XN = 4 * MB;
constexpr size_t OFF_XNF = 6 * MB;
constexpr size_t OFF_KF = 8 * MB;
constexpr size_t OFF_VF = 24 * MB;
constexpr size_t OFF_OF = 40 * MB;
constexpr size_t OFF_KMF = 56 * MB;
constexpr size_t OFF_VMF = 60 * MB;
constexpr size_t OFF_R = 64 * MB;
constexpr size_t OFF_HM = 64 * MB + 65536;
constexpr size_t OFF_UPD = 64 * MB + 131072;
constexpr size_t WS_USED = 65 * MB;
constexpr int C_POLA = 0, C_POLB = 1, C_TMO = 64, C_IDX0 = 128, C_IDX1 = 129;

constexpr unsigned SENT = 0xFFFFFFFFu;
constexpr unsigned SPINCAP = 1u << 20;
constexpr int AUX_SC1 = 16;

struct Params {
    const u16* x; const u16* mem0; const u16* Wq; const u16* Wk; const u16* Wv; const u16* Wo;
    const u16* wr; const u16* wg; const u16* wu; const u16* wd;
    u16* out; u8* ws;
};

__device__ __forceinline__ float u2f(unsigned b) { return __builtin_bit_cast(float, b); }
__device__ __forceinline__ unsigned f2u(float f) { return __builtin_bit_cast(unsigned, f); }
__device__ __forceinline__ float bf2f(unsigned b) { return __builtin_bit_cast(float, b << 16); }
__device__ __forceinline__ unsigned pk2(float a, float b) { bf16x2 p = {(__bf16)a, (__bf16)b}; return __builtin_bit_cast(unsigned, p); }
__device__ __forceinline__ float bfr(float a) { return bf2f(pk2(a, a) & 0xFFFFu); }
__device__ __forceinline__ f32x4 mfma16(v4u a, v4u b, f32x4 c) {
    return __builtin_amdgcn_mfma_f32_16x16x32_bf16(__builtin_bit_cast(bf16x8, a), __builtin_bit_cast(bf16x8, b), c, 0, 0, 0);
}
__device__ __forceinline__ f32x4 smf(v4u a, v8u b, f32x4 c, int idx) {
    return __builtin_amdgcn_smfmac_f32_16x16x64_bf16(__builtin_bit_cast(bf16x8, a), __builtin_bit_cast(bf16x16, b), c, idx, 0, 0);
}
__device__ __forceinline__ v2u trread(LAS u8* p) {
    s16x4 r = __builtin_amdgcn_ds_read_tr16_b64_v4i16((LAS s16x4*)p);
    return __builtin_bit_cast(v2u, r);
}
__device__ __forceinline__ v4u ldsr128(LAS u8* p) { return *(LAS v4u*)p; }
__device__ __forceinline__ void ldsw128(LAS u8* p, v4u v) { *(LAS v4u*)p = v; }
__device__ __forceinline__ void glds16(const void* g, LAS u8* l) {
    __builtin_amdgcn_global_load_lds((const GAS unsigned*)g, (LAS unsigned*)l, 16, 0, 0);
}
#define WAITVM0() asm volatile("s_waitcnt vmcnt(0)" ::: "memory")
#define BAR() do { asm volatile("s_waitcnt lgkmcnt(0)" ::: "memory"); __builtin_amdgcn_s_barrier(); asm volatile("" ::: "memory"); } while (0)
__device__ __forceinline__ int amap(int x, int pol) { return ((x >> 13) << 14) + pol * 8192 + (x & 8191); }
__device__ __forceinline__ float fsw16(float v, bool mx) {
    unsigned u = __builtin_bit_cast(unsigned, v);
    auto r = __builtin_amdgcn_permlane16_swap(u, u, false, false);
    float a = u2f(r[0]), b = u2f(r[1]);
    return mx ? fmaxf(a, b) : a + b;
}
__device__ __forceinline__ float fsw32(float v, bool mx) {
    unsigned u = __builtin_bit_cast(unsigned, v);
    auto r = __builtin_amdgcn_permlane32_swap(u, u, false, false);
    float a = u2f(r[0]), b = u2f(r[1]);
    return mx ? fmaxf(a, b) : a + b;
}
__device__ __forceinline__ float paradd(float a, float b) {
    auto r = __builtin_amdgcn_permlane32_swap(__builtin_bit_cast(unsigned, a), __builtin_bit_cast(unsigned, b), false, false);
    return u2f(r[0]) + u2f(r[1]);
}
__device__ __forceinline__ float dpp_ror8(float v) {
    return __builtin_bit_cast(float, __builtin_amdgcn_update_dpp(0, __builtin_bit_cast(int, v), 0x128, 0xf, 0xf, false));
}
__device__ __forceinline__ __amdgpu_buffer_rsrc_t mkrs(const void* p, unsigned bytes) { return __builtin_amdgcn_make_buffer_rsrc((void*)p, 0, bytes, 0x00020000); }
__device__ __forceinline__ v4u bld128(__amdgpu_buffer_rsrc_t rs, int off) { return __builtin_amdgcn_raw_buffer_load_b128(rs, off, 0, AUX_SC1); }
__device__ __forceinline__ void bst128(__amdgpu_buffer_rsrc_t rs, int off, v4u v) { __builtin_amdgcn_raw_buffer_store_b128(v, rs, off, 0, AUX_SC1); }
__device__ __forceinline__ void bst64(__amdgpu_buffer_rsrc_t rs, int off, v2u v) { __builtin_amdgcn_raw_buffer_store_b64(v, rs, off, 0, AUX_SC1); }

template <int ROWS>
__device__ __forceinline__ void stage_slab128(const u16* src, int rstride, LAS u8* L, int w, int lane) {
#pragma unroll 4
    for (int i = 0; i < ROWS / 16; ++i) {
        const int row0 = (i * 4 + w) * 4;
        const int row = row0 + (lane >> 4), pos = lane & 15, ch = pos ^ (2 * (row & 7));
        glds16(src + (size_t)row * rstride + ch * 8, L + row0 * 256);
    }
}
__device__ __forceinline__ LAS u8* slab_addr(LAS u8* L, int row, int col) {
    return L + row * 256 + (((col >> 3) ^ (2 * (row & 7))) << 4) + ((col & 4) << 1);
}

__global__ void __launch_bounds__(256) k_prep(Params p) {
    const int tid = threadIdx.x, lane = tid & 63, w = tid >> 6, b = blockIdx.x;
    u8* ws = p.ws;
    const v4u wr4 = *(const v4u*)(p.wr + lane * 8);
#pragma unroll
    for (int rr = 0; rr < 2; ++rr) {
        const int row = b * 8 + w * 2 + rr;
        const v4u xv = *(const v4u*)(p.x + (size_t)row * D_ + lane * 8);
        float f[8];
#pragma unroll
        for (int j = 0; j < 4; ++j) { f[2 * j] = bf2f(xv[j] & 0xFFFFu); f[2 * j + 1] = bf2f(xv[j] >> 16); }
        float s = 0.f;
#pragma unroll
        for (int j = 0; j < 8; ++j) s += f[j];
#pragma unroll
        for (int m = 1; m < 64; m <<= 1) s += __shfl_xor(s, m);
        const float mean = s * (1.0f / D_);
        float v = 0.f;
#pragma unroll
        for (int j = 0; j < 8; ++j) { const float d = f[j] - mean; v += d * d; }
#pragma unroll
        for (int m = 1; m < 64; m <<= 1) v += __shfl_xor(v, m);
        const float mb = bfr(mean), vb = bfr(v * (1.0f / D_));
        const float den = bfr(sqrtf(bfr(vb + 1e-5f)));
        float xn[8]; float r = 0.f;
#pragma unroll
        for (int j = 0; j < 8; ++j) { xn[j] = bfr(bfr(f[j] - mb) / den); r += xn[j] * bf2f((j & 1) ? (wr4[j >> 1] >> 16) : (wr4[j >> 1] & 0xFFFFu)); }
#pragma unroll
        for (int m = 1; m < 64; m <<= 1) r += __shfl_xor(r, m);
        v4u o = {pk2(xn[0], xn[1]), pk2(xn[2], xn[3]), pk2(xn[4], xn[5]), pk2(xn[6], xn[7])};
        *(v4u*)(ws + OFF_XN + (size_t)row * 1024 + lane * 16) = o;
        const int seg = row >> 5, tt = (row >> 4) & 1, tok = row & 15, kk = lane >> 2, g = lane & 3;
        *(v4u*)(ws + OFF_XNF + (size_t)((seg * 2 + tt) * 16 + kk) * 1024 + (g * 16 + tok) * 16) = o;
        if (lane == 0) ((float*)(ws + OFF_R))[row] = bfr(r);
    }
    {
        v4u sv = {SENT, SENT, SENT, SENT};
        v4u* dst = (v4u*)(ws + OFF_PART + (size_t)b * 8192);
        dst[tid] = sv; dst[tid + 256] = sv;
    }
    if (b < 8) {
        v4u* dst = (v4u*)(ws + OFF_M + (size_t)b * 8192);
#pragma unroll
        for (int i = 0; i < 2; ++i) {
            const int e = tid + i * 256;
            v4u v = {SENT, SENT, SENT, SENT};
            if ((b >> 1) == 0) {
                const int kb = e >> 6, l = e & 63, gA = l >> 4, pp = (l >> 3) & 1, r = l & 7;
                v = *(const v4u*)(p.mem0 + r * D_ + 64 * kb + 16 * gA + 8 * pp);
#pragma unroll
                for (int j = 0; j < 4; ++j) v[j] = v[j] < 0xFFFFFFFEu ? v[j] : 0xFFFFFFFEu;
            }
            dst[e] = v;
        }
    }
    if (b == 9 && w == 0) {
        gu32* ctl = (gu32*)(ws + OFF_CTL);
        const unsigned xcc = (unsigned)__builtin_amdgcn_s_getreg(20 | (3 << 11)) & 0xfu;
        const __amdgpu_buffer_rsrc_t rs = mkrs(ws, 0x7FFFFFFF);
        unsigned res[2];
#pragma unroll
        for (int pg = 0; pg < 2; ++pg) {
            unsigned long long t[2];
#pragma unroll
            for (int pol = 0; pol < 2; ++pol) {
                const int off = pg == 0 ? (int)(OFF_PART + 2 * MB - 16384 + pol * 8192) : (int)(OFF_M + 0x80000 + pol * 8192);
                unsigned long long best = ~0ull;
                for (int it = 0; it < 3; ++it) {
                    unsigned long long c0, c1;
                    asm volatile("s_memtime %0\n\ts_waitcnt lgkmcnt(0)" : "=s"(c0)::"memory");
                    v4u sv = {SENT, SENT, SENT, SENT};
                    if (lane < 8) bst128(rs, off + lane * 16, sv);
                    WAITVM0();
                    asm volatile("s_memtime %0\n\ts_waitcnt lgkmcnt(0)" : "=s"(c1)::"memory");
                    best = (c1 - c0) < best ? (c1 - c0) : best;
                }
                t[pol] = best;
            }
            unsigned nearpol = t[0] <= t[1] ? 0u : 1u;
            res[pg] = nearpol ^ ((xcc >> 2) & 1u);
        }
        if (lane == 0) { ctl[C_POLA] = res[0]; ctl[C_POLB] = res[1]; ctl[C_TMO] = 0u; }
    }
}

constexpr int LDS_KV = 128 * 1024;
__device__ __forceinline__ void load_dense_w(const u16* W, int h, LAS u8* L, int w, int lane, v4u (&wf)[2][16]) {
    stage_slab128<512>(W + h * DH_, 4096, L, w, lane);
    WAITVM0();
    BAR();
    const int g = lane >> 4, q = (lane >> 2) & 3, pp = lane & 3;
#pragma unroll
    for (int t = 0; t < 2; ++t)
#pragma unroll
        for (int kk = 0; kk < 16; ++kk) {
            const int col = 32 * w + 16 * t + 4 * pp;
            v2u a = trread(slab_addr(L, 32 * kk + 8 * g + q, col));
            v2u b = trread(slab_addr(L, 32 * kk + 8 * g + 4 + q, col));
            wf[t][kk] = v4u{a.x, a.y, b.x, b.y};
        }
    BAR();
}
__device__ __forceinline__ void stage_xnf(const u8* ws, int seg, LAS u8* dst, int w, int lane) {
#pragma unroll
    for (int i = 0; i < 8; ++i) {
        const int pc = i * 4 + w;
        glds16(ws + OFF_XNF + (size_t)seg * 32768 + pc * 1024 + lane * 16, dst + pc * 1024);
    }
}
__device__ void kv_role(const Params& p, LAS u8* L, int which, int h, int seg0, int step) {
    const int tid = threadIdx.x, lane = tid & 63, w = __builtin_amdgcn_readfirstlane(tid >> 6);
    v4u wf[2][16];
    load_dense_w(which ? p.Wv : p.Wk, h, L, w, lane, wf);
    const __amdgpu_buffer_rsrc_t rs = mkrs(p.ws + (which ? OFF_VF : OFF_KF), 16 * MB);
    int buf = 0;
    stage_xnf(p.ws, seg0, L, w, lane);
    for (int seg = seg0; seg < NSEG; seg += step) {
        WAITVM0();
        BAR();
        if (seg + step < NSEG) stage_xnf(p.ws, seg + step, L + (buf ^ 1) * 32768, w, lane);
        LAS u8* X = L + buf * 32768;
        f32x4 acc[2][2];
#pragma unroll
        for (int a = 0; a < 2; ++a)
#pragma unroll
            for (int b = 0; b < 2; ++b) acc[a][b] = f32x4{0.f, 0.f, 0.f, 0.f};
        if (which == 0) {
#pragma unroll
            for (int kk = 0; kk < 16; ++kk)
#pragma unroll
                for (int tt = 0; tt < 2; ++tt) {
                    const v4u xf = ldsr128(X + (tt * 16 + kk) * 1024 + lane * 16);
                    acc[0][tt] = mfma16(wf[0][kk], xf, acc[0][tt]);
                    acc[1][tt] = mfma16(wf[1][kk], xf, acc[1][tt]);
                }
#pragma unroll
            for (int tt = 0; tt < 2; ++tt) {
                v4u o = {pk2(acc[0][tt][0], acc[0][tt][1]), pk2(acc[0][tt][2], acc[0][tt][3]), pk2(acc[1][tt][0], acc[1][tt][1]), pk2(acc[1][tt][2], acc[1][tt][3])};
                bst128(rs, (((seg * 32 + h) * 2 + tt) * 4 + w) * 1024 + lane * 16, o);
            }
        } else {
#pragma unroll
            for (int kk = 0; kk < 16; ++kk)
#pragma unroll
                for (int tt = 0; tt < 2; ++tt) {
                    const v4u xf = ldsr128(X + (tt * 16 + kk) * 1024 + lane * 16);
                    acc[0][tt] = mfma16(xf, wf[0][kk], acc[0][tt]);
                    acc[1][tt] = mfma16(xf, wf[1][kk], acc[1][tt]);
                }
#pragma unroll
            for (int t = 0; t < 2; ++t) {
                v4u o = {pk2(acc[t][0][0], acc[t][0][1]), pk2(acc[t][0][2], acc[t][0][3]), pk2(acc[t][1][0], acc[t][1][1]), pk2(acc[t][1][2], acc[t][1][3])};
                bst128(rs, ((seg * 32 + h) * 8 + 2 * w + t) * 1024 + lane * 16, o);
            }
        }
        buf ^= 1;
    }
    WAITVM0();
}
__global__ void __launch_bounds__(256, 1) k_kv(Params p) {
    extern __shared__ __attribute__((aligned(16))) u8 smem[];
    const int b = blockIdx.x;
    kv_role(p, (LAS u8*)smem, b & 1, (b >> 1) & 31, b >> 6, 2);
}

constexpr int L_QT = 128 * 1024, L_KT = L_QT + 2048, L_OX = L_KT + 2048, LDS_CHAIN = L_OX + 4096;
__device__ void chain_role(const Params& p, LAS u8* L, int c) {
    const int tid = threadIdx.x, lane = tid & 63, w = __builtin_amdgcn_readfirstlane(tid >> 6);
    const int h = (c & 3) * 8 + (c >> 4), part = (c >> 2) & 3;
    const int g = lane >> 4, j16 = lane & 15, q4 = (lane >> 2) & 3, pp = lane & 3;
    u8* ws = p.ws;
    gu32* ctl = (gu32*)(ws + OFF_CTL);
    const int polA = __builtin_amdgcn_readfirstlane(ctl[C_POLA] & 1u), polB = __builtin_amdgcn_readfirstlane(ctl[C_POLB] & 1u);
    v8u wq[2][8], wk[2][8];
    v4u wo[2][4];
    auto load_sparse_b = [&](const u16* W, v8u (&wf)[2][8]) {
        stage_slab128<512>(W + h * DH_, 4096, L, w, lane);
        WAITVM0();
        BAR();
#pragma unroll
        for (int t = 0; t < 2; ++t)
#pragma unroll
            for (int kb = 0; kb < 8; ++kb) {
                const int col = 32 * w + 16 * t + 4 * pp;
                const int r0 = 64 * kb + 16 * (g >> 1) + 4 * (g & 1) + q4;
                v2u x0 = trread(slab_addr(L, r0, col)), y0 = trread(slab_addr(L, r0 + 8, col));
                v2u x1 = trread(slab_addr(L, r0 + 32, col)), y1 = trread(slab_addr(L, r0 + 40, col));
                wf[t][kb] = v8u{x0.x, y0.x, x0.y, y0.y, x1.x, y1.x, x1.y, y1.y};
            }
        BAR();
    };
    load_sparse_b(p.Wq, wq);
    load_sparse_b(p.Wk, wk);
    {
        stage_slab128<128>(p.Wo + (size_t)h * DH_ * D_ + part * 128, D_, L, w, lane);
        WAITVM0();
        BAR();
#pragma unroll
        for (int t = 0; t < 2; ++t)
#pragma unroll
            for (int ks = 0; ks < 4; ++ks) {
                const int col = 32 * w + 16 * t + 4 * pp;
                v2u a = trread(slab_addr(L, 32 * ks + 4 * g + q4, col)), b = trread(slab_addr(L, 32 * ks + 16 + 4 * g + q4, col));
                wo[t][ks] = v4u{a.x, a.y, b.x, b.y};
            }
        BAR();
    }
    {
        v8u wv[2][8];
        load_sparse_b(p.Wv, wv);
#pragma unroll
        for (int t = 0; t < 2; ++t)
#pragma unroll
            for (int kb = 0; kb < 8; ++kb) {
                LAS u8* d = L + (((w * 2 + t) * 8 + kb) * 2) * 1024 + lane * 16;
                ldsw128(d, v4u{wv[t][kb][0], wv[t][kb][1], wv[t][kb][2], wv[t][kb][3]});
                ldsw128(d + 1024, v4u{wv[t][kb][4], wv[t][kb][5], wv[t][kb][6], wv[t][kb][7]});
            }
        BAR();
    }
    const int sidx = (j16 < 8) ? 0x4444 : 0xEEEE;
    const __amdgpu_buffer_rsrc_t rsP = mkrs(ws + OFF_PART, 2 * MB);
    const __amdgpu_buffer_rsrc_t rsM = mkrs(ws + OFF_M, 65536);
    const __amdgpu_buffer_rsrc_t rsK = mkrs(ws + OFF_KF, 16 * MB);
    const __amdgpu_buffer_rsrc_t rsV = mkrs(ws + OFF_VF, 16 * MB);
    const __amdgpu_buffer_rsrc_t rsKM = mkrs(ws + OFF_KMF, 4 * MB);
    const __amdgpu_buffer_rsrc_t rsVM = mkrs(ws + OFF_VMF, 4 * MB);
    const float cs = 0.08838834764831845f * 1.4426950408889634f;
    v4u kt[2][4], vt[2];
    auto load_kv = [&](int s) {
#pragma unroll
        for (int tt = 0; tt < 2; ++tt)
#pragma unroll
            for (int ks = 0; ks < 4; ++ks) kt[tt][ks] = bld128(rsK, (((s * 32 + h) * 2 + tt) * 4 + ks) * 1024 + lane * 16);
#pragma unroll
        for (int t = 0; t < 2; ++t) vt[t] = bld128(rsV, ((s * 32 + h) * 8 + 2 * w + t) * 1024 + lane * 16);
    };
    load_kv(0);
    bool dead = false;
    for (int s = 0; s < NSEG; ++s) {
        v4u mf[8];
        {
            unsigned sp = 0;
            const int o0 = amap((s & 3) * 8192 + lane * 16, polB);
            for (;;) {
                asm volatile("" ::: "memory");
                bool ok = true;
#pragma unroll
                for (int kb = 0; kb < 8; ++kb) mf[kb] = bld128(rsM, o0 + kb * 1024);
#pragma unroll
                for (int kb = 0; kb < 8; ++kb) ok &= (mf[kb].x != SENT) & (mf[kb].y != SENT) & (mf[kb].z != SENT) & (mf[kb].w != SENT);
                if (__all(ok)) break;
                if (++sp > SPINCAP) { dead = true; break; }
            }
        }
        if (w == 0 && s >= 1 && lane < 8) bst64(rsM, amap(((s - 1) & 3) * 8192 + c * 64 + lane * 8, polB), v2u{SENT, SENT});
        f32x4 aq[2], ak[2], av[2];
#pragma unroll
        for (int t = 0; t < 2; ++t) { aq[t] = f32x4{0.f, 0.f, 0.f, 0.f}; ak[t] = aq[t]; av[t] = aq[t]; }
#pragma unroll
        for (int kb = 0; kb < 8; ++kb) {
            ak[0] = smf(mf[kb], wk[0][kb], ak[0], sidx);
            ak[1] = smf(mf[kb], wk[1][kb], ak[1], sidx);
            aq[0] = smf(mf[kb], wq[0][kb], aq[0], sidx);
            aq[1] = smf(mf[kb], wq[1][kb], aq[1], sidx);
        }
        {
            float qv[4], kv[4];
#pragma unroll
            for (int r = 0; r < 4; ++r) { qv[r] = paradd(aq[0][r], aq[1][r]); kv[r] = paradd(ak[0][r], ak[1][r]); }
            const int f = 32 * w + 16 * (lane >> 5) + j16, gp = (lane >> 4) & 1;
            *(LAS v2u*)(L + L_QT + f * 16 + gp * 8) = v2u{pk2(qv[0], qv[1]), pk2(qv[2], qv[3])};
            *(LAS v2u*)(L + L_KT + f * 16 + gp * 8) = v2u{pk2(kv[0], kv[1]), pk2(kv[2], kv[3])};
        }
#pragma unroll
        for (int kb = 0; kb < 8; ++kb)
#pragma unroll
            for (int t = 0; t < 2; ++t) {
                LAS u8* d = L + (((w * 2 + t) * 8 + kb) * 2) * 1024 + lane * 16;
                const v4u lo = ldsr128(d), hi = ldsr128(d + 1024);
                av[t] = smf(mf[kb], v8u{lo.x, lo.y, lo.z, lo.w, hi.x, hi.y, hi.z, hi.w}, av[t], sidx);
            }
        BAR();
        f32x4 st[3];
        v4u kfw;
#pragma unroll
        for (int t = 0; t < 3; ++t) st[t] = f32x4{0.f, 0.f, 0.f, 0.f};
#pragma unroll
        for (int ks = 0; ks < 4; ++ks) {
            const int fo = (32 * ks + 4 * g + q4) * 16 + (pp & 1) * 8;
            const v2u qa = trread(L + L_QT + fo), qb = trread(L + L_QT + fo + 256);
            const v2u ka = trread(L + L_KT + fo), kb2 = trread(L + L_KT + fo + 256);
            const v4u qf = {qa.x, qa.y, qb.x, qb.y}, kf = {ka.x, ka.y, kb2.x, kb2.y};
            if (ks == w) kfw = kf;
            st[0] = mfma16(kf, qf, st[0]);
            st[1] = mfma16(kt[0][ks], qf, st[1]);
            st[2] = mfma16(kt[1][ks], qf, st[2]);
        }
        v4u vm;
        {
            float vv[4];
#pragma unroll
            for (int r = 0; r < 4; ++r) vv[r] = paradd(av[0][r], av[1][r]);
            vm = v4u{pk2(vv[0], vv[1]), pk2(vv[2], vv[3]), 0u, 0u};
        }
        if (part == 0) { if (j16 < 8) bst128(rsKM, (s * 32 + h) * 2048 + w * 512 + g * 128 + j16 * 16, kfw); }
        else if (part == 1) bst64(rsVM, (s * 32 + h) * 2048 + w * 512 + lane * 8, v2u{vm.x, vm.y});
        if (s == NSEG - 1) break;
        float mx = fmaxf(fmaxf(fmaxf(st[0][0], st[0][1]), fmaxf(st[0][2], st[0][3])), fmaxf(fmaxf(fmaxf(st[1][0], st[1][1]), fmaxf(st[1][2], st[1][3])), fmaxf(fmaxf(st[2][0], st[2][1]), fmaxf(st[2][2], st[2][3]))));
        mx = fsw16(mx, true);
        mx = fsw32(mx, true);
        float e[3][4];
#pragma unroll
        for (int t = 0; t < 3; ++t)
#pragma unroll
            for (int r = 0; r < 4; ++r) e[t][r] = __builtin_amdgcn_exp2f((st[t][r] - mx) * cs);
        float sum = (e[1][0] + e[1][1]) + (e[1][2] + e[1][3]) + (e[2][0] + e[2][1]) + (e[2][2] + e[2][3]);
        const float s0 = (e[0][0] + e[0][1]) + (e[0][2] + e[0][3]);
        sum += (g < 2) ? s0 : 0.f;
        sum = fsw16(sum, false);
        sum = fsw32(sum, false);
        const float inv = __builtin_amdgcn_rcpf(sum);
        const v4u pt = {pk2(e[1][0] * inv, e[1][1] * inv), pk2(e[1][2] * inv, e[1][3] * inv), pk2(e[2][0] * inv, e[2][1] * inv), pk2(e[2][2] * inv, e[2][3] * inv)};
        const unsigned pm0 = pk2(e[0][0] * inv, e[0][1] * inv), pm1 = pk2(e[0][2] * inv, e[0][3] * inv);
        const v4u plo = {g < 2 ? pm0 : 0u, g < 2 ? pm1 : 0u, 0u, 0u}, phi = {g < 2 ? 0u : pm0, g < 2 ? 0u : pm1, 0u, 0u};
        f32x4 ot[2];
        ot[0] = mfma16(vt[0], pt, f32x4{0.f, 0.f, 0.f, 0.f});
        ot[1] = mfma16(vt[1], pt, f32x4{0.f, 0.f, 0.f, 0.f});
        ot[0] = mfma16(vm, plo, ot[0]);
        ot[1] = mfma16(vm, phi, ot[1]);
        load_kv(s + 1);
        ldsw128(L + L_OX + w * 1024 + lane * 16, v4u{pk2(ot[0][0], ot[0][1]), pk2(ot[0][2], ot[0][3]), pk2(ot[1][0], ot[1][1]), pk2(ot[1][2], ot[1][3])});
        BAR();
        f32x4 ap[2];
        ap[0] = f32x4{0.f, 0.f, 0.f, 0.f}; ap[1] = ap[0];
#pragma unroll
        for (int ks = 0; ks < 4; ++ks) {
            const v4u of = ldsr128(L + L_OX + ks * 1024 + lane * 16);
            ap[0] = mfma16(wo[0][ks], of, ap[0]);
            ap[1] = mfma16(wo[1][ks], of, ap[1]);
        }
        {
            const int t = j16 >> 3, row = j16 & 7;
            v4u o;
#pragma unroll
            for (int r = 0; r < 4; ++r) { const unsigned u = f2u(t ? ap[1][r] : ap[0][r]); o[r] = u < 0xFFFFFFFEu ? u : 0xFFFFFFFEu; }
            const int c0 = 128 * part + 32 * w + 16 * t;
            const int u8i = (c0 >> 3) + (g >> 1), red = 2 * u8i + (row >> 2);
            const int off = (s & 1) * 524288 + red * 4096 + h * 128 + (row & 3) * 32 + (g & 1) * 16;
            bst128(rsP, amap(off, polA), o);
        }
        if (w == 0) {
            v4u x[4];
            unsigned sp = 0;
            const int o0 = amap((s & 1) * 524288 + c * 4096 + lane * 16, polA);
            for (;;) {
                asm volatile("" ::: "memory");
                bool ok = true;
#pragma unroll
                for (int k = 0; k < 4; ++k) x[k] = bld128(rsP, o0 + k * 1024);
#pragma unroll
                for (int k = 0; k < 4; ++k) ok &= (x[k].x != SENT) & (x[k].y != SENT) & (x[k].z != SENT) & (x[k].w != SENT);
                if (__all(ok)) break;
                if (++sp > SPINCAP) { dead = true; break; }
            }
            float sm[4];
#pragma unroll
            for (int r = 0; r < 4; ++r) {
                float a = (u2f(x[0][r]) + u2f(x[1][r])) + (u2f(x[2][r]) + u2f(x[3][r]));
                a += dpp_ror8(a);
                a = fsw16(a, false);
                a = fsw32(a, false);
                sm[r] = a;
            }
            unsigned o0w = pk2(sm[0], sm[1]), o1w = pk2(sm[2], sm[3]);
            o0w = o0w < 0xFFFFFFFEu ? o0w : 0xFFFFFFFEu; o1w = o1w < 0xFFFFFFFEu ? o1w : 0xFFFFFFFEu;
            if (lane < 8) bst64(rsM, amap(((s + 1) & 3) * 8192 + c * 64 + lane * 8, polB), v2u{o0w, o1w});
#pragma unroll
            for (int k = 0; k < 4; ++k) bst128(rsP, o0 + k * 1024, v4u{SENT, SENT, SENT, SENT});
        }
        if (dead) break;
    }
    if (dead && lane == 0) __hip_atomic_store(ctl + C_TMO, 1u, RLX_AGENT);
    WAITVM0();
}
__global__ void __launch_bounds__(256, 1) k_chain(Params p) {
    extern __shared__ __attribute__((aligned(16))) u8 smem[];
    const int b = blockIdx.x;
    if ((b & 7) >= 4) return;
    chain_role(p, (LAS u8*)smem, (b >> 3) * 4 + (b & 3));
}

constexpr int L_QX = 128 * 1024, LDS_QA = L_QX + 8192;
__device__ void qa_role(const Params& p, LAS u8* L, int h, int seg0, int step) {
    const int tid = threadIdx.x, lane = tid & 63, w = __builtin_amdgcn_readfirstlane(tid >> 6);
    const int g = lane >> 4, j16 = lane & 15;
    v4u wf[2][16];
    load_dense_w(p.Wq, h, L, w, lane, wf);
    u8* ws = p.ws;
    const __amdgpu_buffer_rsrc_t rsK = mkrs(ws + OFF_KF, 16 * MB);
    const __amdgpu_buffer_rsrc_t rsV = mkrs(ws + OFF_VF, 16 * MB);
    const __amdgpu_buffer_rsrc_t rsO = mkrs(ws + OFF_OF, 16 * MB);
    const __amdgpu_buffer_rsrc_t rsKM = mkrs(ws + OFF_KMF, 4 * MB);
    const __amdgpu_buffer_rsrc_t rsVM = mkrs(ws + OFF_VMF, 4 * MB);
    const float cs = 0.08838834764831845f * 1.4426950408889634f;
    const int tt = w & 1, eh = w >> 1;
    int buf = 0;
    stage_xnf(ws, seg0, L, w, lane);
    for (int seg = seg0; seg < NSEG; seg += step) {
        WAITVM0();
        BAR();
        if (seg + step < NSEG) stage_xnf(ws, seg + step, L + (buf ^ 1) * 32768, w, lane);
        LAS u8* X = L + buf * 32768;
        f32x4 acc[2][2];
#pragma unroll
        for (int a = 0; a < 2; ++a)
#pragma unroll
            for (int b = 0; b < 2; ++b) acc[a][b] = f32x4{0.f, 0.f, 0.f, 0.f};
#pragma unroll
        for (int kk = 0; kk < 16; ++kk)
#pragma unroll
            for (int t2 = 0; t2 < 2; ++t2) {
                const v4u xf = ldsr128(X + (t2 * 16 + kk) * 1024 + lane * 16);
                acc[0][t2] = mfma16(wf[0][kk], xf, acc[0][t2]);
                acc[1][t2] = mfma16(wf[1][kk], xf, acc[1][t2]);
            }
#pragma unroll
        for (int t2 = 0; t2 < 2; ++t2)
            ldsw128(L + L_QX + (t2 * 4 + w) * 1024 + lane * 16, v4u{pk2(acc[0][t2][0], acc[0][t2][1]), pk2(acc[0][t2][2], acc[0][t2][3]), pk2(acc[1][t2][0], acc[1][t2][1]), pk2(acc[1][t2][2], acc[1][t2][3])});
        v4u kt[2][4], km[4], vt[4];
        v2u vmm[4];
#pragma unroll
        for (int ks = 0; ks < 4; ++ks) {
            kt[0][ks] = bld128(rsK, (((seg * 32 + h) * 2 + 0) * 4 + ks) * 1024 + lane * 16);
            kt[1][ks] = bld128(rsK, (((seg * 32 + h) * 2 + 1) * 4 + ks) * 1024 + lane * 16);
            km[ks] = bld128(rsKM, (seg * 32 + h) * 2048 + ks * 512 + g * 128 + (j16 & 7) * 16);
        }
#pragma unroll
        for (int e = 0; e < 4; ++e) {
            const int et = 4 * eh + e;
            vt[e] = bld128(rsV, ((seg * 32 + h) * 8 + et) * 1024 + lane * 16);
            vmm[e] = __builtin_amdgcn_raw_buffer_load_b64(rsVM, (seg * 32 + h) * 2048 + (et >> 1) * 512 + ((et & 1) * 32 + (g & 1) * 16 + j16) * 8, 0, AUX_SC1);
        }
        BAR();
        f32x4 st[3];
#pragma unroll
        for (int t = 0; t < 3; ++t) st[t] = f32x4{0.f, 0.f, 0.f, 0.f};
#pragma unroll
        for (int ks = 0; ks < 4; ++ks) {
            const v4u qf = ldsr128(L + L_QX + (tt * 4 + ks) * 1024 + lane * 16);
            st[0] = mfma16(km[ks], qf, st[0]);
            st[1] = mfma16(kt[0][ks], qf, st[1]);
            st[2] = mfma16(kt[1][ks], qf, st[2]);
        }
        const int qi = 16 * tt + j16;
        const float NEG = -3.0e38f;
#pragma unroll
        for (int r = 0; r < 4; ++r) {
            if (g >= 2) st[0][r] = NEG;
            if (4 * g + r > qi) st[1][r] = NEG;
            if (16 + 4 * g + r > qi) st[2][r] = NEG;
        }
        float mx = NEG;
#pragma unroll
        for (int t = 0; t < 3; ++t)
#pragma unroll
            for (int r = 0; r < 4; ++r) mx = fmaxf(mx, st[t][r]);
        mx = fsw16(mx, true);
        mx = fsw32(mx, true);
        float e[3][4], sum = 0.f;
#pragma unroll
        for (int t = 0; t < 3; ++t)
#pragma unroll
            for (int r = 0; r < 4; ++r) { e[t][r] = st[t][r] == NEG ? 0.f : __builtin_amdgcn_exp2f((st[t][r] - mx) * cs); sum += e[t][r]; }
        sum = fsw16(sum, false);
        sum = fsw32(sum, false);
        const float inv = __builtin_amdgcn_rcpf(sum);
        const v4u pt = {pk2(e[1][0] * inv, e[1][1] * inv), pk2(e[1][2] * inv, e[1][3] * inv), pk2(e[2][0] * inv, e[2][1] * inv), pk2(e[2][2] * inv, e[2][3] * inv)};
        const v4u pm = {pk2(e[0][0] * inv, e[0][1] * inv), pk2(e[0][2] * inv, e[0][3] * inv), 0u, 0u};
        f32x4 ot[4];
#pragma unroll
        for (int e4 = 0; e4 < 4; ++e4) {
            ot[e4] = mfma16(vt[e4], pt, f32x4{0.f, 0.f, 0.f, 0.f});
            const v4u vmf = {g < 2 ? vmm[e4].x : 0u, g < 2 ? vmm[e4].y : 0u, 0u, 0u};
            ot[e4] = mfma16(vmf, pm, ot[e4]);
        }
#pragma unroll
        for (int pr = 0; pr < 2; ++pr) {
            const v4u o = {pk2(ot[2 * pr][0], ot[2 * pr][1]), pk2(ot[2 * pr][2], ot[2 * pr][3]), pk2(ot[2 * pr + 1][0], ot[2 * pr + 1][1]), pk2(ot[2 * pr + 1][2], ot[2 * pr + 1][3])};
            bst128(rsO, ((seg * 2 + tt) * 128 + h * 4 + 2 * eh + pr) * 1024 + lane * 16, o);
        }
        buf ^= 1;
    }
    WAITVM0();
}
__global__ void __launch_bounds__(256, 1) k_qa(Params p) {
    extern __shared__ __attribute__((aligned(16))) u8 smem[];
    const int b = blockIdx.x;
    qa_role(p, (LAS u8*)smem, b & 31, b >> 5, 2);
}

__global__ void __launch_bounds__(256) k_moe1(Params p) {
    __shared__ float sv[4][2]; __shared__ int si[4][2]; __shared__ int sel[2];
    __shared__ float red[2][2][16][17];
    const int tid = threadIdx.x, lane = tid & 63, w = tid >> 6;
    const float* R = (const float*)(p.ws + OFF_R);
    float v0 = -INFINITY, v1 = -INFINITY; int i0 = 0x7FFFFFFF, i1 = 0x7FFFFFFF;
#define BETTER(a, ia, b, ib) ((a) > (b) || ((a) == (b) && (ia) < (ib)))
#define INS(V, I) do { const float v_ = (V); const int i_ = (I); const bool b0_ = BETTER(v_, i_, v0, i0), b1_ = BETTER(v_, i_, v1, i1); \
        v1 = b0_ ? v0 : (b1_ ? v_ : v1); i1 = b0_ ? i0 : (b1_ ? i_ : i1); v0 = b0_ ? v_ : v0; i0 = b0_ ? i_ : i0; } while (0)
    for (int t = tid; t < S_; t += 256) INS(R[t], t);
#pragma unroll
    for (int m = 1; m < 64; m <<= 1) {
        const float a0 = __shfl_xor(v0, m), a1 = __shfl_xor(v1, m); const int b0 = __shfl_xor(i0, m), b1 = __shfl_xor(i1, m);
        INS(a0, b0); INS(a1, b1);
    }
    if (lane == 0) { sv[w][0] = v0; sv[w][1] = v1; si[w][0] = i0; si[w][1] = i1; }
    __syncthreads();
    if (tid == 0) {
        v0 = sv[0][0]; v1 = sv[0][1]; i0 = si[0][0]; i1 = si[0][1];
        for (int k = 1; k < 4; ++k) { INS(sv[k][0], si[k][0]); INS(sv[k][1], si[k][1]); }
        sel[0] = i0; sel[1] = i1;
        if (blockIdx.x == 0) { gu32* ctl = (gu32*)(p.ws + OFF_CTL); ctl[C_IDX0] = i0; ctl[C_IDX1] = i1; }
    }
    __syncthreads();
    const u16* XN = (const u16*)(p.ws + OFF_XN);
    const int col = blockIdx.x * 16 + (tid & 15), kq = tid >> 4;
    float ag[2] = {0.f, 0.f}, au[2] = {0.f, 0.f};
    for (int k = kq * 32; k < kq * 32 + 32; ++k) {
        const float wg = bf2f(p.wg[(size_t)k * F_ + col]), wu = bf2f(p.wu[(size_t)k * F_ + col]);
        const float x0 = bf2f(XN[(size_t)sel[0] * D_ + k]), x1 = bf2f(XN[(size_t)sel[1] * D_ + k]);
        ag[0] += x0 * wg; ag[1] += x1 * wg; au[0] += x0 * wu; au[1] += x1 * wu;
    }
    red[0][0][tid & 15][kq] = ag[0]; red[0][1][tid & 15][kq] = ag[1]; red[1][0][tid & 15][kq] = au[0]; red[1][1][tid & 15][kq] = au[1];
    __syncthreads();
    if (tid < 32) {
        const int c = tid & 15, t = tid >> 4;
        float gsum = 0.f, usum = 0.f;
        for (int k = 0; k < 16; ++k) { gsum += red[0][t][c][k]; usum += red[1][t][c][k]; }
        const float gb = bfr(gsum), ub = bfr(usum);
        const float sg = bfr(gb / (1.0f + __expf(-gb)));
        ((float*)(p.ws + OFF_HM))[t * F_ + blockIdx.x * 16 + c] = bfr(sg * ub);
    }
}
__global__ void __launch_bounds__(256) k_moe2(Params p) {
    __shared__ float red[2][16][17];
    const int tid = threadIdx.x;
    const float* HM = (const float*)(p.ws + OFF_HM);
    const int col = blockIdx.x * 16 + (tid & 15), kq = tid >> 4;
    float a0 = 0.f, a1 = 0.f;
    for (int k = kq * 112; k < kq * 112 + 112; ++k) {
        const float wd = bf2f(p.wd[(size_t)k * D_ + col]);
        a0 += HM[k] * wd; a1 += HM[F_ + k] * wd;
    }
    red[0][tid & 15][kq] = a0; red[1][tid & 15][kq] = a1;
    __syncthreads();
    if (tid < 32) {
        const int c = tid & 15, t = tid >> 4;
        float s = 0.f;
        for (int k = 0; k < 16; ++k) s += red[t][c][k];
        gu32* ctl = (gu32*)(p.ws + OFF_CTL);
        const int idx = ctl[t ? C_IDX1 : C_IDX0];
        const float r = ((const float*)(p.ws + OFF_R))[idx];
        const float sg = bfr(1.0f / (1.0f + __expf(-r)));
        ((float*)(p.ws + OFF_UPD))[t * D_ + blockIdx.x * 16 + c] = bfr(bfr(s) * sg);
    }
}

constexpr int L_RED = 128 * 1024, LDS_O = L_RED + 8192;
__device__ void o_role(const Params& p, LAS u8* L, int cp, int tt, int seg0, int step) {
    const int tid = threadIdx.x, lane = tid & 63, w = __builtin_amdgcn_readfirstlane(tid >> 6);
    const int g = lane >> 4, j16 = lane & 15, q4 = (lane >> 2) & 3, pp = lane & 3;
    u8* ws = p.ws;
    v4u wf[2][32];
#pragma unroll
    for (int half = 0; half < 2; ++half) {
#pragma unroll 4
        for (int i = 0; i < 32; ++i) {
            const int row0 = (i * 4 + w) * 16;
            glds16(p.Wo + (size_t)(2048 * half + row0 + (lane >> 2)) * D_ + 32 * cp + (lane & 3) * 8, L + row0 * 64);
        }
        WAITVM0();
        BAR();
        if ((w >> 1) == half) {
#pragma unroll
            for (int t = 0; t < 2; ++t)
#pragma unroll
                for (int k = 0; k < 32; ++k) {
                    const int rb = 1024 * (w & 1) + 32 * k;
                    v2u a = trread(L + (rb + 4 * g + q4) * 64 + t * 32 + pp * 8);
                    v2u b = trread(L + (rb + 16 + 4 * g + q4) * 64 + t * 32 + pp * 8);
                    wf[t][k] = v4u{a.x, a.y, b.x, b.y};
                }
        }
        BAR();
    }
    const __amdgpu_buffer_rsrc_t rsO = mkrs(ws + OFF_OF, 16 * MB);
    gu32* ctl = (gu32*)(ws + OFF_CTL);
    const int i0 = ctl[C_IDX0], i1 = ctl[C_IDX1];
    for (int seg = seg0; seg < NSEG; seg += step) {
        f32x4 acc[2];
        acc[0] = f32x4{0.f, 0.f, 0.f, 0.f}; acc[1] = acc[0];
#pragma unroll
        for (int k0 = 0; k0 < 32; k0 += 8) {
            v4u of[8];
#pragma unroll
            for (int k = 0; k < 8; ++k) of[k] = bld128(rsO, ((seg * 2 + tt) * 128 + 32 * w + k0 + k) * 1024 + lane * 16);
#pragma unroll
            for (int k = 0; k < 8; ++k) { acc[0] = mfma16(wf[0][k0 + k], of[k], acc[0]); acc[1] = mfma16(wf[1][k0 + k], of[k], acc[1]); }
        }
        *(LAS f32x4*)(L + L_RED + (w * 2 + 0) * 1024 + lane * 16) = acc[0];
        *(LAS f32x4*)(L + L_RED + (w * 2 + 1) * 1024 + lane * 16) = acc[1];
        BAR();
        if (w < 2) {
            f32x4 s = *(LAS f32x4*)(L + L_RED + (0 * 2 + w) * 1024 + lane * 16);
#pragma unroll
            for (int k = 1; k < 4; ++k) s += *(LAS f32x4*)(L + L_RED + (k * 2 + w) * 1024 + lane * 16);
            const int tok = seg * 32 + tt * 16 + j16, col = 32 * cp + 16 * w + 4 * g;
            const v2u xv = *(const v2u*)(p.x + (size_t)tok * D_ + col);
            const v2u nv = *(const v2u*)(ws + OFF_XN + (size_t)tok * 1024 + col * 2);
            float o[4];
#pragma unroll
            for (int r = 0; r < 4; ++r) {
                const float xf = bf2f((r & 1) ? (xv[r >> 1] >> 16) : (xv[r >> 1] & 0xFFFFu));
                float nf = bf2f((r & 1) ? (nv[r >> 1] >> 16) : (nv[r >> 1] & 0xFFFFu));
                float upd = 0.f;
                if (tok == i0) upd += ((const float*)(ws + OFF_UPD))[col + r];
                if (tok == i1) upd = bfr(upd + ((const float*)(ws + OFF_UPD))[D_ + col + r]);
                nf = bfr(nf + upd);
                o[r] = bfr(xf + bfr(s[r])) + nf;
            }
            *(v2u*)(p.out + (size_t)tok * D_ + col) = v2u{pk2(o[0], o[1]), pk2(o[2], o[3])};
        }
        BAR();
    }
}
__global__ void __launch_bounds__(256, 1) k_o(Params p) {
    extern __shared__ __attribute__((aligned(16))) u8 smem[];
    const int b = blockIdx.x;
    o_role(p, (LAS u8*)smem, b & 15, (b >> 4) & 1, b >> 5, 2);
}

}

extern "C" void kernel_launch(void* const* d_in, const int* in_sizes, int n_in, void* d_out, int out_size, void* d_ws, size_t ws_size, hipStream_t stream) {
    Params p;
    p.x = (const u16*)d_in[0]; p.mem0 = (const u16*)d_in[1]; p.Wq = (const u16*)d_in[2]; p.Wk = (const u16*)d_in[3]; p.Wv = (const u16*)d_in[4]; p.Wo = (const u16*)d_in[5];
    p.wr = (const u16*)d_in[8]; p.wg = (const u16*)d_in[9]; p.wu = (const u16*)d_in[10]; p.wd = (const u16*)d_in[11];
    p.out = (u16*)d_out;
    p.ws = (u8*)(((uintptr_t)d_ws + 2 * MB - 1) & ~(uintptr_t)(2 * MB - 1));
    (void)hipFuncSetAttribute((const void*)k_kv, hipFuncAttributeMaxDynamicSharedMemorySize, LDS_KV);
    (void)hipFuncSetAttribute((const void*)k_chain, hipFuncAttributeMaxDynamicSharedMemorySize, LDS_CHAIN);
    (void)hipFuncSetAttribute((const void*)k_qa, hipFuncAttributeMaxDynamicSharedMemorySize, LDS_QA);
    (void)hipFuncSetAttribute((const void*)k_o, hipFuncAttributeMaxDynamicSharedMemorySize, LDS_O);
    k_prep<<<256, 256, 0, stream>>>(p);
    k_kv<<<128, 256, LDS_KV, stream>>>(p);
    k_chain<<<256, 256, LDS_CHAIN, stream>>>(p);
    k_qa<<<64, 256, LDS_QA, stream>>>(p);
    k_moe1<<<F_ / 16, 256, 0, stream>>>(p);
    k_moe2<<<D_ / 16, 256, 0, stream>>>(p);
    k_o<<<64, 256, LDS_O, stream>>>(p);
}
```

```cpp
#include <hip/hip_runtime.h>
#include <hip/hip_bf16.h>
#include <stdint.h>

#define GAS __attribute__((address_space(1)))
#define LAS __attribute__((address_space(3)))
typedef unsigned short u16;
typedef unsigned char u8;
typedef unsigned v4u __attribute__((ext_vector_type(4)));
typedef unsigned v2u __attribute__((ext_vector_type(2)));
typedef unsigned v8u __attribute__((ext_vector_type(8)));
typedef float f32x4 __attribute__((ext_vector_type(4)));
typedef __bf16 bf16x2 __attribute__((ext_vector_type(2)));
typedef __bf16 bf16x8 __attribute__((ext_vector_type(8)));
typedef __bf16 bf16x16 __attribute__((ext_vector_type(16)));
typedef short s16x4 __attribute__((ext_vector_type(4)));
typedef GAS unsigned gu32;
#define RLX_AGENT __ATOMIC_RELAXED, __HIP_MEMORY_SCOPE_AGENT

constexpr int NCAL = 4;
constexpr int SLP = 4;
constexpr int DAV = 19, DBV = 19;
namespace {

constexpr int S_ = 2048, D_ = 512, H_ = 32, DH_ = 128, NSEG = 64, F_ = 1792;
constexpr size_t MB = 1u << 20;
constexpr size_t OFF_PART = 0;
constexpr size_t OFF_M = 2 * MB;
constexpr size_t OFF_CTL = 64 * MB + 262144;
constexpr size_t OFF_XN = 4 * MB;
constexpr size_t OFF_XNF = 6 * MB;
constexpr size_t OFF_KVF = 8 * MB;
constexpr size_t OFF_OF = 40 * MB;
constexpr size_t OFF_KMF = 56 * MB;
constexpr size_t OFF_VMF = 60 * MB;
constexpr size_t OFF_R = 64 * MB;
constexpr size_t OFF_HM = 64 * MB + 65536;
constexpr size_t OFF_UPD = 64 * MB + 131072;
constexpr size_t WS_USED = 65 * MB;
constexpr int C_POLA = 0, C_POLB = 1, C_TMO = 64, C_IDX0 = 128, C_IDX1 = 129;
constexpr int C_CAL = 96;
constexpr int C_KV = 1024, C_KM = 8192, C_OC = 12288, C_MOE1 = 15360, C_MOE2 = 15392;

constexpr unsigned SENT = 0xFFFFFFFFu;
constexpr unsigned SPINCAP = 1u << 20;
constexpr int AUX_SC1 = 16;

struct Params {
    const u16* x; const u16* mem0; const u16* Wq; const u16* Wk; const u16* Wv; const u16* Wo;
    const u16* wr; const u16* wg; const u16* wu; const u16* wd;
    u16* out; u8* ws;
};

__device__ __forceinline__ float u2f(unsigned b) { return __builtin_bit_cast(float, b); }
__device__ __forceinline__ unsigned f2u(float f) { return __builtin_bit_cast(unsigned, f); }
__device__ __forceinline__ float bf2f(unsigned b) { return __builtin_bit_cast(float, b << 16); }
__device__ __forceinline__ unsigned pk2(float a, float b) { bf16x2 p = {(__bf16)a, (__bf16)b}; return __builtin_bit_cast(unsigned, p); }
__device__ __forceinline__ float bfr(float a) { return bf2f(pk2(a, a) & 0xFFFFu); }
__device__ __forceinline__ f32x4 mfma16(v4u a, v4u b, f32x4 c) {
    return __builtin_amdgcn_mfma_f32_16x16x32_bf16(__builtin_bit_cast(bf16x8, a), __builtin_bit_cast(bf16x8, b), c, 0, 0, 0);
}
__device__ __forceinline__ f32x4 smf(v4u a, v8u b, f32x4 c, int idx) {
    return __builtin_amdgcn_smfmac_f32_16x16x64_bf16(__builtin_bit_cast(bf16x8, a), __builtin_bit_cast(bf16x16, b), c, idx, 0, 0);
}
__device__ __forceinline__ v2u trread(LAS u8* p) {
    s16x4 r = __builtin_amdgcn_ds_read_tr16_b64_v4i16((LAS s16x4*)p);
    return __builtin_bit_cast(v2u, r);
}
__device__ __forceinline__ v4u ldsr128(LAS u8* p) { return *(LAS v4u*)p; }
__device__ __forceinline__ void ldsw128(LAS u8* p, v4u v) { *(LAS v4u*)p = v; }
__device__ __forceinline__ void glds16(const void* g, LAS u8* l) {
    __builtin_amdgcn_global_load_lds((const GAS unsigned*)g, (LAS unsigned*)l, 16, 0, 0);
}
#define WAITVM0() asm volatile("s_waitcnt vmcnt(0)" ::: "memory")
#define BAR() do { asm volatile("s_waitcnt lgkmcnt(0)" ::: "memory"); __builtin_amdgcn_s_barrier(); asm volatile("" ::: "memory"); } while (0)
__device__ __forceinline__ int amap(int x, int pol) { return ((x >> 13) << 14) + pol * 8192 + (x & 8191); }
__device__ __forceinline__ float fsw16(float v, bool mx) {
    unsigned u = __builtin_bit_cast(unsigned, v);
    auto r = __builtin_amdgcn_permlane16_swap(u, u, false, false);
    float a = u2f(r[0]), b = u2f(r[1]);
    return mx ? fmaxf(a, b) : a + b;
}
__device__ __forceinline__ float fsw32(float v, bool mx) {
    unsigned u = __builtin_bit_cast(unsigned, v);
    auto r = __builtin_amdgcn_permlane32_swap(u, u, false, false);
    float a = u2f(r[0]), b = u2f(r[1]);
    return mx ? fmaxf(a, b) : a + b;
}
__device__ __forceinline__ float paradd(float a, float b) {
    auto r = __builtin_amdgcn_permlane32_swap(__builtin_bit_cast(unsigned, a), __builtin_bit_cast(unsigned, b), false, false);
    return u2f(r[0]) + u2f(r[1]);
}
__device__ __forceinline__ float dpp_ror8(float v) {
    return __builtin_bit_cast(float, __builtin_amdgcn_update_dpp(0, __builtin_bit_cast(int, v), 0x128, 0xf, 0xf, false));
}
__device__ __forceinline__ __amdgpu_buffer_rsrc_t mkrs(const void* p, unsigned bytes) { return __builtin_amdgcn_make_buffer_rsrc((void*)p, 0, bytes, 0x00020000); }
__device__ __forceinline__ v4u bld128(__amdgpu_buffer_rsrc_t rs, int off) { return __builtin_amdgcn_raw_buffer_load_b128(rs, off, 0, AUX_SC1); }
__device__ __forceinline__ void bst128(__amdgpu_buffer_rsrc_t rs, int off, v4u v) { __builtin_amdgcn_raw_buffer_store_b128(v, rs, off, 0, AUX_SC1); }
__device__ __forceinline__ void bst64(__amdgpu_buffer_rsrc_t rs, int off, v2u v) { __builtin_amdgcn_raw_buffer_store_b64(v, rs, off, 0, AUX_SC1); }

template <int ROWS>
__device__ __forceinline__ void stage_slab128(const u16* src, int rstride, LAS u8* L, int w, int lane) {
#pragma unroll 4
    for (int i = 0; i < ROWS / 16; ++i) {
        const int row0 = (i * 4 + w) * 4;
        const int row = row0 + (lane >> 4), pos = lane & 15, ch = pos ^ (2 * (row & 7));
        glds16(src + (size_t)row * rstride + ch * 8, L + row0 * 256);
    }
}
__device__ __forceinline__ LAS u8* slab_addr(LAS u8* L, int row, int col) {
    return L + row * 256 + (((col >> 3) ^ (2 * (row & 7))) << 4) + ((col & 4) << 1);
}

__device__ __forceinline__ float wsum(float v) {
    v += __builtin_bit_cast(float, __builtin_amdgcn_update_dpp(0, __builtin_bit_cast(int, v), 0xB1, 0xf, 0xf, false));
    v += __builtin_bit_cast(float, __builtin_amdgcn_update_dpp(0, __builtin_bit_cast(int, v), 0x4E, 0xf, 0xf, false));
    v += __builtin_bit_cast(float, __builtin_amdgcn_update_dpp(0, __builtin_bit_cast(int, v), 0x124, 0xf, 0xf, false));
    v += dpp_ror8(v);
    v = fsw16(v, false);
    v = fsw32(v, false);
    return v;
}
__device__ __forceinline__ void calibrate4(const Params& p, int slot, int w, int lane);
__global__ void __launch_bounds__(256) k_prep(Params p) {
    const int tid = threadIdx.x, lane = tid & 63, w = tid >> 6, bi = blockIdx.x;
    u8* ws = p.ws;
    if (bi < 32 && (bi & 3) == 0) { if (w == 0) calibrate4(p, (bi >> 2) & 1, bi >> 3, lane); return; }
    const int b = bi < 32 ? bi - 1 - (bi >> 2) : bi - 8;
    if (b >= 512) return;
    {
        const int row = b * 4 + w;
        const v4u xv = *(const v4u*)(p.x + (size_t)row * D_ + lane * 8);
        const v4u wr4 = *(const v4u*)(p.wr + lane * 8);
        float f[8];
#pragma unroll
        for (int j = 0; j < 4; ++j) { f[2 * j] = bf2f(xv[j] & 0xFFFFu); f[2 * j + 1] = bf2f(xv[j] >> 16); }
        float s = ((f[0] + f[1]) + (f[2] + f[3])) + ((f[4] + f[5]) + (f[6] + f[7]));
        const float mean = wsum(s) * (1.0f / D_);
        float v = 0.f;
#pragma unroll
        for (int j = 0; j < 8; ++j) { const float d = f[j] - mean; v += d * d; }
        v = wsum(v);
        const float mb = bfr(mean), vb = bfr(v * (1.0f / D_));
        const float den = bfr(sqrtf(bfr(vb + 1e-5f)));
        float xn[8]; float r = 0.f;
#pragma unroll
        for (int j = 0; j < 8; ++j) { xn[j] = bfr(bfr(f[j] - mb) / den); r += xn[j] * bf2f((j & 1) ? (wr4[j >> 1] >> 16) : (wr4[j >> 1] & 0xFFFFu)); }
        v4u o = {pk2(xn[0], xn[1]), pk2(xn[2], xn[3]), pk2(xn[4], xn[5]), pk2(xn[6], xn[7])};
        *(v4u*)(ws + OFF_XN + (size_t)row * 1024 + lane * 16) = o;
        const int seg = row >> 5, tt = (row >> 4) & 1, tok = row & 15, kk = lane >> 2, g = lane & 3;
        *(v4u*)(ws + OFF_XNF + (size_t)((seg * 2 + tt) * 16 + kk) * 1024 + (g * 16 + tok) * 16) = o;
        r = wsum(r);
        if (lane == 0) ((float*)(ws + OFF_R))[row] = bfr(r);
    }
    {
        v4u sv = {SENT, SENT, SENT, SENT};
        ((v4u*)(ws + OFF_PART + (size_t)b * 4096))[tid] = sv;
        if (b < 256) ((v4u*)(ws + 3 * MB + (size_t)b * 4096))[tid] = sv;
    }
    if (b >= 256 && b < 320) {
        const int pb = (b - 256) >> 1, e = tid + (b & 1) * 256;
        v4u v = {SENT, SENT, SENT, SENT};
        if ((pb >> 3) == 0) {
            const int c = e >> 2, u = c >> 1, vv = c & 1, rr = e & 3;
            v = *(const v4u*)(p.mem0 + (4 * vv + rr) * D_ + 8 * u);
#pragma unroll
            for (int j = 0; j < 4; ++j) v[j] = v[j] < 0xFFFFFFFEu ? v[j] : 0xFFFFFFFEu;
        }
        ((v4u*)(ws + OFF_M + (size_t)pb * 8192))[e] = v;
    }
    if (b >= 320 && b < 336) { gu32* ctl = (gu32*)(ws + OFF_CTL); const int i = 1024 + (b - 320) * 1024 + tid * 4; ctl[i] = 0u; ctl[i + 1] = 0u; ctl[i + 2] = 0u; ctl[i + 3] = 0u; }
    if (b == 336 && tid < 96) { gu32* ctl = (gu32*)(ws + OFF_CTL); ctl[tid] = 0u; }
}

__device__ __forceinline__ void calibrate4(const Params& p, int slot, int w, int lane) {
    gu32* ctl = (gu32*)(p.ws + OFF_CTL);
    const unsigned xcc = (unsigned)__builtin_amdgcn_s_getreg(20 | (3 << 11)) & 0xfu;
    const __amdgpu_buffer_rsrc_t rs = mkrs(p.ws, 0x7FFFFFFF);
    const int pg = w >> 1, pol = w & 1;
    const int off0 = pg == 0 ? (int)(OFF_PART + 2 * MB - 16384 + pol * 8192) : (int)(OFF_M + 0x80000 + pol * 8192);
    unsigned best = ~0u;
    for (int it = 0; it < NCAL; ++it) {
        const int off = off0 + it * 2048 + 896;
        unsigned long long c0, c1;
        asm volatile("s_memtime %0\n\ts_waitcnt lgkmcnt(0)" : "=s"(c0)::"memory");
        v4u sv = {SENT, SENT, SENT, SENT};
        if (lane < 8) bst128(rs, off + lane * 16, sv);
        WAITVM0();
        asm volatile("s_memtime %0\n\ts_waitcnt lgkmcnt(0)" : "=s"(c1)::"memory");
        best = (unsigned)(c1 - c0) < best ? (unsigned)(c1 - c0) : best;
    }
    if (lane == 0) ctl[C_CAL + slot * 4 + w] = best * 2u + ((xcc >> 2) & 1u);
}

__device__ __forceinline__ void load_dense_w(const u16* W, int h, LAS u8* L, int w, int lane, v4u (&wf)[2][16]) {
    stage_slab128<512>(W + h * DH_, 4096, L, w, lane);
    WAITVM0();
    BAR();
    const int g = lane >> 4, q = (lane >> 2) & 3, pp = lane & 3;
#pragma unroll
    for (int t = 0; t < 2; ++t)
#pragma unroll
        for (int kk = 0; kk < 16; ++kk) {
            const int col = 32 * w + 16 * t + 4 * pp;
            v2u a = trread(slab_addr(L, 32 * kk + 8 * g + q, col));
            v2u b = trread(slab_addr(L, 32 * kk + 8 * g + 4 + q, col));
            wf[t][kk] = v4u{a.x, a.y, b.x, b.y};
        }
    BAR();
}
__device__ __forceinline__ void stage_xnf(const u8* ws, int seg, LAS u8* dst, int w, int lane) {
#pragma unroll
    for (int i = 0; i < 8; ++i) {
        const int pc = i * 4 + w;
        glds16(ws + OFF_XNF + (size_t)seg * 32768 + pc * 1024 + lane * 16, dst + pc * 1024);
    }
}
template <bool WA>
__device__ __forceinline__ void gemm_seg(const v4u (&wf)[2][16], LAS u8* X, int lane, f32x4 (&acc)[2][2]) {
#pragma unroll
    for (int a = 0; a < 2; ++a)
#pragma unroll
        for (int b = 0; b < 2; ++b) acc[a][b] = f32x4{0.f, 0.f, 0.f, 0.f};
    v4u xf[2][8];
#pragma unroll
    for (int k = 0; k < 4; ++k)
#pragma unroll
        for (int tt = 0; tt < 2; ++tt) xf[0][k * 2 + tt] = ldsr128(X + (tt * 16 + k) * 1024 + lane * 16);
#pragma unroll
    for (int gq = 0; gq < 4; ++gq) {
        if (gq < 3) {
#pragma unroll
            for (int k = 0; k < 4; ++k)
#pragma unroll
                for (int tt = 0; tt < 2; ++tt) xf[(gq + 1) & 1][k * 2 + tt] = ldsr128(X + (tt * 16 + (gq + 1) * 4 + k) * 1024 + lane * 16);
        }
        __builtin_amdgcn_sched_barrier(0);
#pragma unroll
        for (int k = 0; k < 4; ++k)
#pragma unroll
            for (int tt = 0; tt < 2; ++tt) {
                const v4u x = xf[gq & 1][k * 2 + tt];
                if (WA) { acc[0][tt] = mfma16(wf[0][gq * 4 + k], x, acc[0][tt]); acc[1][tt] = mfma16(wf[1][gq * 4 + k], x, acc[1][tt]); }
                else { acc[0][tt] = mfma16(x, wf[0][gq * 4 + k], acc[0][tt]); acc[1][tt] = mfma16(x, wf[1][gq * 4 + k], acc[1][tt]); }
            }
        __builtin_amdgcn_sched_barrier(0);
    }
}
__device__ __forceinline__ bool spin_ge(gu32* word, unsigned need, gu32* tmo, bool hurry = false) {
    unsigned sp = 0;
    while ((unsigned)__builtin_amdgcn_readfirstlane(__hip_atomic_load(word, RLX_AGENT)) < need) {
        if (hurry) __builtin_amdgcn_s_sleep(1); else __builtin_amdgcn_s_sleep(SLP);
        if (++sp > SPINCAP) { __hip_atomic_store(tmo, 1u, RLX_AGENT); return false; }
    }
    return true;
}

__device__ __forceinline__ void kv_role(const Params& p, LAS u8* L, int which, int h, int par) {
    const int tid = threadIdx.x, lane = tid & 63, w = __builtin_amdgcn_readfirstlane(tid >> 6);
    gu32* ctl = (gu32*)(p.ws + OFF_CTL);
    gu32* flag = ctl + C_KV + ((h * 2 + which) * 2 + par) * 32;
    v4u wf[2][16];
    load_dense_w(which ? p.Wv : p.Wk, h, L, w, lane, wf);
    const __amdgpu_buffer_rsrc_t rs = mkrs(p.ws + OFF_KVF, 32 * MB);
    stage_xnf(p.ws, par, L, w, lane);
    for (int it = 0; it < 32; ++it) {
        const int seg = 2 * it + par;
        if (it < 2) WAITVM0(); else asm volatile("s_waitcnt vmcnt(2)" ::: "memory");
        BAR();
        if (tid == 0 && it >= 1) __hip_atomic_store(flag, (unsigned)(it >= 2 && it != 1 ? it - 1 : it), RLX_AGENT);
        if (it + 1 < 32) stage_xnf(p.ws, seg + 2, L + ((it + 1) & 1) * 32768, w, lane);
        f32x4 acc[2][2];
        if (which == 0) {
            gemm_seg<true>(wf, L + (it & 1) * 32768, lane, acc);
#pragma unroll
            for (int tt = 0; tt < 2; ++tt) {
                v4u o = {pk2(acc[0][tt][0], acc[0][tt][1]), pk2(acc[0][tt][2], acc[0][tt][3]), pk2(acc[1][tt][0], acc[1][tt][1]), pk2(acc[1][tt][2], acc[1][tt][3])};
                bst128(rs, ((seg * 32 + h) * 16 + tt * 4 + w) * 1024 + lane * 16, o);
            }
        } else {
            gemm_seg<false>(wf, L + (it & 1) * 32768, lane, acc);
#pragma unroll
            for (int t = 0; t < 2; ++t) {
                v4u o = {pk2(acc[t][0][0], acc[t][0][1]), pk2(acc[t][0][2], acc[t][0][3]), pk2(acc[t][1][0], acc[t][1][1]), pk2(acc[t][1][2], acc[t][1][3])};
                bst128(rs, ((seg * 32 + h) * 16 + 8 + t * 4 + w) * 1024 + lane * 16, o);
            }
        }
    }
    WAITVM0();
    BAR();
    if (tid == 0) __hip_atomic_store(flag, 32u, RLX_AGENT);
}

#define CHAIN_ASM_TEXT \
  "s_mov_b64 s[98:99], %0\n" \
  "s_mov_b32 s79, %5\n" \
  "s_mov_b32 s80, %6\n" \
  "s_mov_b32 s78, %7\n" \
  "s_mov_b32 s77, %8\n" \
  "s_lshl_b32 s81, %9, 13\n" \
  "s_lshl_b32 s82, %10, 13\n" \
  "s_mov_b32 s48, %11\n" \
  "s_mov_b32 s49, %12\n" \
  "s_mov_b32 s59, 0\n" \
  "s_add_u32 s36, s98, 0x200000\n" \
  "s_addc_u32 s37, s99, 0\n" \
  "s_and_b32 s37, s37, 0xffff\n" \
  "s_mov_b32 s38, 0x40000\n" \
  "s_mov_b32 s39, 0x20000\n" \
  "s_add_u32 s40, s98, 0x0\n" \
  "s_addc_u32 s41, s99, 0\n" \
  "s_and_b32 s41, s41, 0xffff\n" \
  "s_mov_b32 s42, 0x400000\n" \
  "s_mov_b32 s43, 0x20000\n" \
  "s_add_u32 s44, s98, 0x800000\n" \
  "s_addc_u32 s45, s99, 0\n" \
  "s_and_b32 s45, s45, 0xffff\n" \
  "s_mov_b32 s46, 0x2000000\n" \
  "s_mov_b32 s47, 0x20000\n" \
  "s_mov_b32 s88, 0x3800000\n" \
  "s_cmp_eq_u32 s78, 1\n" \
  "s_cselect_b32 s88, 0x3c00000, s88\n" \
  "s_cmp_ge_u32 s78, 2\n" \
  "s_cselect_b32 s88, 0x4100000, s88\n" \
  "s_add_u32 s52, s98, s88\n" \
  "s_addc_u32 s53, s99, 0\n" \
  "s_and_b32 s53, s53, 0xffff\n" \
  "s_mov_b32 s54, 0x400000\n" \
  "s_mov_b32 s55, 0x20000\n" \
  "s_add_u32 s92, s98, 0x4040100\n" \
  "s_addc_u32 s93, s99, 0\n" \
  "s_mov_b32 s83, 0\n" \
  "v_mbcnt_lo_u32_b32 v95, -1, 0\n" \
  "v_mbcnt_hi_u32_b32 v95, -1, v95\n" \
  "v_lshrrev_b32 v0, 4, v95\n" \
  "v_and_b32 v1, 15, v95\n" \
  "v_bfe_u32 v2, v95, 2, 2\n" \
  "v_and_b32 v3, 3, v95\n" \
  "v_lshlrev_b32 v32, 4, v95\n" \
  "s_lshl_b32 s88, s77, 10\n" \
  "v_add_u32 v33, s88, v32\n" \
  "s_and_b32 s89, s77, 1\n" \
  "s_lshl_b32 s89, s89, 2\n" \
  "v_add_u32 v4, s89, v0\n" \
  "v_and_b32 v4, 7, v4\n" \
  "v_lshlrev_b32 v4, 1, v4\n" \
  "v_xor_b32 v4, v4, v1\n" \
  "v_lshlrev_b32 v4, 4, v4\n" \
  "v_lshl_add_u32 v8, v0, 13, v4\n" \
  "v_lshl_add_u32 v9, v0, 10, v4\n" \
  "v_and_b32 v4, 1, v0\n" \
  "v_lshl_add_u32 v4, v4, 2, v2\n" \
  "v_lshlrev_b32 v5, 1, v4\n" \
  "v_lshrrev_b32 v6, 1, v3\n" \
  "s_lshl_b32 s89, s77, 2\n" \
  "v_add_u32 v6, s89, v6\n" \
  "v_add_u32 v7, 0, v6\n" \
  "v_xor_b32 v7, v7, v5\n" \
  "v_lshlrev_b32 v7, 4, v7\n" \
  "v_and_b32 v16, 1, v3\n" \
  "v_lshl_add_u32 v7, v16, 3, v7\n" \
  "v_lshrrev_b32 v16, 1, v0\n" \
  "v_lshl_add_u32 v16, v16, 4, v4\n" \
  "v_lshl_add_u32 v10, v16, 8, v7\n" \
  "v_add_u32 v12, 0x10000, v10\n" \
  "v_lshl_add_u32 v16, v0, 2, v2\n" \
  "v_lshl_add_u32 v14, v16, 8, v7\n" \
  "v_add_u32 v7, 2, v6\n" \
  "v_xor_b32 v7, v7, v5\n" \
  "v_lshlrev_b32 v7, 4, v7\n" \
  "v_and_b32 v16, 1, v3\n" \
  "v_lshl_add_u32 v7, v16, 3, v7\n" \
  "v_lshrrev_b32 v16, 1, v0\n" \
  "v_lshl_add_u32 v16, v16, 4, v4\n" \
  "v_lshl_add_u32 v11, v16, 8, v7\n" \
  "v_add_u32 v13, 0x10000, v11\n" \
  "v_lshl_add_u32 v16, v0, 2, v2\n" \
  "v_lshl_add_u32 v15, v16, 8, v7\n" \
  "s_mov_b64 s[60:61], %2\n" \
  "s_and_b32 s61, s61, 0xffff\n" \
  "s_mov_b32 s62, 0x400000\n" \
  "s_mov_b32 s63, 0x20000\n" \
  "s_lshl_b32 s88, s77, 10\n" \
  "s_mul_i32 s89, s77, 32768\n" \
  "s_add_u32 m0, s88, 0\n" \
  "s_add_u32 s90, s89, 0\n" \
  "buffer_load_dwordx4 v8, s[60:63], s90 offen lds\n" \
  "s_add_u32 m0, s88, 4096\n" \
  "s_add_u32 s90, s89, 131072\n" \
  "buffer_load_dwordx4 v8, s[60:63], s90 offen lds\n" \
  "s_add_u32 m0, s88, 8192\n" \
  "s_add_u32 s90, s89, 262144\n" \
  "buffer_load_dwordx4 v8, s[60:63], s90 offen lds\n" \
  "s_add_u32 m0, s88, 12288\n" \
  "s_add_u32 s90, s89, 393216\n" \
  "buffer_load_dwordx4 v8, s[60:63], s90 offen lds\n" \
  "s_add_u32 m0, s88, 16384\n" \
  "s_add_u32 s90, s89, 524288\n" \
  "buffer_load_dwordx4 v8, s[60:63], s90 offen lds\n" \
  "s_add_u32 m0, s88, 20480\n" \
  "s_add_u32 s90, s89, 655360\n" \
  "buffer_load_dwordx4 v8, s[60:63], s90 offen lds\n" \
  "s_add_u32 m0, s88, 24576\n" \
  "s_add_u32 s90, s89, 786432\n" \
  "buffer_load_dwordx4 v8, s[60:63], s90 offen lds\n" \
  "s_add_u32 m0, s88, 28672\n" \
  "s_add_u32 s90, s89, 917504\n" \
  "buffer_load_dwordx4 v8, s[60:63], s90 offen lds\n" \
  "s_mov_b64 s[60:61], %2\n" \
  "s_and_b32 s61, s61, 0xffff\n" \
  "s_mov_b32 s62, 0x400000\n" \
  "s_mov_b32 s63, 0x20000\n" \
  "s_lshl_b32 s88, s77, 10\n" \
  "s_mul_i32 s89, s77, 32768\n" \
  "s_add_u32 m0, s88, 32768\n" \
  "s_add_u32 s90, s89, 1048576\n" \
  "buffer_load_dwordx4 v8, s[60:63], s90 offen lds\n" \
  "s_add_u32 m0, s88, 36864\n" \
  "s_add_u32 s90, s89, 1179648\n" \
  "buffer_load_dwordx4 v8, s[60:63], s90 offen lds\n" \
  "s_add_u32 m0, s88, 40960\n" \
  "s_add_u32 s90, s89, 1310720\n" \
  "buffer_load_dwordx4 v8, s[60:63], s90 offen lds\n" \
  "s_add_u32 m0, s88, 45056\n" \
  "s_add_u32 s90, s89, 1441792\n" \
  "buffer_load_dwordx4 v8, s[60:63], s90 offen lds\n" \
  "s_add_u32 m0, s88, 49152\n" \
  "s_add_u32 s90, s89, 1572864\n" \
  "buffer_load_dwordx4 v8, s[60:63], s90 offen lds\n" \
  "s_add_u32 m0, s88, 53248\n" \
  "s_add_u32 s90, s89, 1703936\n" \
  "buffer_load_dwordx4 v8, s[60:63], s90 offen lds\n" \
  "s_add_u32 m0, s88, 57344\n" \
  "s_add_u32 s90, s89, 1835008\n" \
  "buffer_load_dwordx4 v8, s[60:63], s90 offen lds\n" \
  "s_add_u32 m0, s88, 61440\n" \
  "s_add_u32 s90, s89, 1966080\n" \
  "buffer_load_dwordx4 v8, s[60:63], s90 offen lds\n" \
  "s_mov_b64 s[60:61], %2\n" \
  "s_and_b32 s61, s61, 0xffff\n" \
  "s_mov_b32 s62, 0x400000\n" \
  "s_mov_b32 s63, 0x20000\n" \
  "s_lshl_b32 s88, s77, 10\n" \
  "s_mul_i32 s89, s77, 32768\n" \
  "s_add_u32 m0, s88, 65536\n" \
  "s_add_u32 s90, s89, 2097152\n" \
  "buffer_load_dwordx4 v8, s[60:63], s90 offen lds\n" \
  "s_add_u32 m0, s88, 69632\n" \
  "s_add_u32 s90, s89, 2228224\n" \
  "buffer_load_dwordx4 v8, s[60:63], s90 offen lds\n" \
  "s_add_u32 m0, s88, 73728\n" \
  "s_add_u32 s90, s89, 2359296\n" \
  "buffer_load_dwordx4 v8, s[60:63], s90 offen lds\n" \
  "s_add_u32 m0, s88, 77824\n" \
  "s_add_u32 s90, s89, 2490368\n" \
  "buffer_load_dwordx4 v8, s[60:63], s90 offen lds\n" \
  "s_add_u32 m0, s88, 81920\n" \
  "s_add_u32 s90, s89, 2621440\n" \
  "buffer_load_dwordx4 v8, s[60:63], s90 offen lds\n" \
  "s_add_u32 m0, s88, 86016\n" \
  "s_add_u32 s90, s89, 2752512\n" \
  "buffer_load_dwordx4 v8, s[60:63], s90 offen lds\n" \
  "s_add_u32 m0, s88, 90112\n" \
  "s_add_u32 s90, s89, 2883584\n" \
  "buffer_load_dwordx4 v8, s[60:63], s90 offen lds\n" \
  "s_add_u32 m0, s88, 94208\n" \
  "s_add_u32 s90, s89, 3014656\n" \
  "buffer_load_dwordx4 v8, s[60:63], s90 offen lds\n" \
  "s_mov_b64 s[60:61], %2\n" \
  "s_and_b32 s61, s61, 0xffff\n" \
  "s_mov_b32 s62, 0x400000\n" \
  "s_mov_b32 s63, 0x20000\n" \
  "s_lshl_b32 s88, s77, 10\n" \
  "s_mul_i32 s89, s77, 32768\n" \
  "s_add_u32 m0, s88, 98304\n" \
  "s_add_u32 s90, s89, 3145728\n" \
  "buffer_load_dwordx4 v8, s[60:63], s90 offen lds\n" \
  "s_add_u32 m0, s88, 102400\n" \
  "s_add_u32 s90, s89, 3276800\n" \
  "buffer_load_dwordx4 v8, s[60:63], s90 offen lds\n" \
  "s_add_u32 m0, s88, 106496\n" \
  "s_add_u32 s90, s89, 3407872\n" \
  "buffer_load_dwordx4 v8, s[60:63], s90 offen lds\n" \
  "s_add_u32 m0, s88, 110592\n" \
  "s_add_u32 s90, s89, 3538944\n" \
  "buffer_load_dwordx4 v8, s[60:63], s90 offen lds\n" \
  "s_add_u32 m0, s88, 114688\n" \
  "s_add_u32 s90, s89, 3670016\n" \
  "buffer_load_dwordx4 v8, s[60:63], s90 offen lds\n" \
  "s_add_u32 m0, s88, 118784\n" \
  "s_add_u32 s90, s89, 3801088\n" \
  "buffer_load_dwordx4 v8, s[60:63], s90 offen lds\n" \
  "s_add_u32 m0, s88, 122880\n" \
  "s_add_u32 s90, s89, 3932160\n" \
  "buffer_load_dwordx4 v8, s[60:63], s90 offen lds\n" \
  "s_add_u32 m0, s88, 126976\n" \
  "s_add_u32 s90, s89, 4063232\n" \
  "buffer_load_dwordx4 v8, s[60:63], s90 offen lds\n" \
  "s_waitcnt vmcnt(24)\n" \
  "s_barrier\n" \
  "ds_read_b64_tr_b16 v[16:17], v10 offset:0\n" \
  "ds_read_b64_tr_b16 v[18:19], v10 offset:2048\n" \
  "ds_read_b64_tr_b16 v[20:21], v10 offset:8192\n" \
  "ds_read_b64_tr_b16 v[22:23], v10 offset:10240\n" \
  "ds_read_b64_tr_b16 v[24:25], v10 offset:16384\n" \
  "ds_read_b64_tr_b16 v[26:27], v10 offset:18432\n" \
  "ds_read_b64_tr_b16 v[28:29], v10 offset:24576\n" \
  "ds_read_b64_tr_b16 v[30:31], v10 offset:26624\n" \
  "s_waitcnt lgkmcnt(4)\n" \
  "v_accvgpr_write_b32 a128, v16\n" \
  "v_accvgpr_write_b32 a129, v18\n" \
  "v_accvgpr_write_b32 a130, v17\n" \
  "v_accvgpr_write_b32 a131, v19\n" \
  "v_accvgpr_write_b32 a132, v20\n" \
  "v_accvgpr_write_b32 a133, v22\n" \
  "v_accvgpr_write_b32 a134, v21\n" \
  "v_accvgpr_write_b32 a135, v23\n" \
  "ds_read_b64_tr_b16 v[16:17], v11 offset:0\n" \
  "ds_read_b64_tr_b16 v[18:19], v11 offset:2048\n" \
  "ds_read_b64_tr_b16 v[20:21], v11 offset:8192\n" \
  "ds_read_b64_tr_b16 v[22:23], v11 offset:10240\n" \
  "s_waitcnt lgkmcnt(4)\n" \
  "v_accvgpr_write_b32 a136, v24\n" \
  "v_accvgpr_write_b32 a137, v26\n" \
  "v_accvgpr_write_b32 a138, v25\n" \
  "v_accvgpr_write_b32 a139, v27\n" \
  "v_accvgpr_write_b32 a140, v28\n" \
  "v_accvgpr_write_b32 a141, v30\n" \
  "v_accvgpr_write_b32 a142, v29\n" \
  "v_accvgpr_write_b32 a143, v31\n" \
  "ds_read_b64_tr_b16 v[24:25], v11 offset:16384\n" \
  "ds_read_b64_tr_b16 v[26:27], v11 offset:18432\n" \
  "ds_read_b64_tr_b16 v[28:29], v11 offset:24576\n" \
  "ds_read_b64_tr_b16 v[30:31], v11 offset:26624\n" \
  "s_waitcnt lgkmcnt(4)\n" \
  "v_accvgpr_write_b32 a192, v16\n" \
  "v_accvgpr_write_b32 a193, v18\n" \
  "v_accvgpr_write_b32 a194, v17\n" \
  "v_accvgpr_write_b32 a195, v19\n" \
  "v_accvgpr_write_b32 a196, v20\n" \
  "v_accvgpr_write_b32 a197, v22\n" \
  "v_accvgpr_write_b32 a198, v21\n" \
  "v_accvgpr_write_b32 a199, v23\n" \
  "s_waitcnt lgkmcnt(0)\n" \
  "v_accvgpr_write_b32 a200, v24\n" \
  "v_accvgpr_write_b32 a201, v26\n" \
  "v_accvgpr_write_b32 a202, v25\n" \
  "v_accvgpr_write_b32 a203, v27\n" \
  "v_accvgpr_write_b32 a204, v28\n" \
  "v_accvgpr_write_b32 a205, v30\n" \
  "v_accvgpr_write_b32 a206, v29\n" \
  "v_accvgpr_write_b32 a207, v31\n" \
  "s_barrier\n" \
  "s_mov_b64 s[60:61], %1\n" \
  "s_and_b32 s61, s61, 0xffff\n" \
  "s_mov_b32 s62, 0x400000\n" \
  "s_mov_b32 s63, 0x20000\n" \
  "s_lshl_b32 s88, s77, 10\n" \
  "s_mul_i32 s89, s77, 32768\n" \
  "s_add_u32 m0, s88, 0\n" \
  "s_add_u32 s90, s89, 0\n" \
  "buffer_load_dwordx4 v8, s[60:63], s90 offen lds\n" \
  "s_add_u32 m0, s88, 4096\n" \
  "s_add_u32 s90, s89, 131072\n" \
  "buffer_load_dwordx4 v8, s[60:63], s90 offen lds\n" \
  "s_add_u32 m0, s88, 8192\n" \
  "s_add_u32 s90, s89, 262144\n" \
  "buffer_load_dwordx4 v8, s[60:63], s90 offen lds\n" \
  "s_add_u32 m0, s88, 12288\n" \
  "s_add_u32 s90, s89, 393216\n" \
  "buffer_load_dwordx4 v8, s[60:63], s90 offen lds\n" \
  "s_add_u32 m0, s88, 16384\n" \
  "s_add_u32 s90, s89, 524288\n" \
  "buffer_load_dwordx4 v8, s[60:63], s90 offen lds\n" \
  "s_add_u32 m0, s88, 20480\n" \
  "s_add_u32 s90, s89, 655360\n" \
  "buffer_load_dwordx4 v8, s[60:63], s90 offen lds\n" \
  "s_add_u32 m0, s88, 24576\n" \
  "s_add_u32 s90, s89, 786432\n" \
  "buffer_load_dwordx4 v8, s[60:63], s90 offen lds\n" \
  "s_add_u32 m0, s88, 28672\n" \
  "s_add_u32 s90, s89, 917504\n" \
  "buffer_load_dwordx4 v8, s[60:63], s90 offen lds\n" \
  "s_waitcnt vmcnt(24)\n" \
  "s_barrier\n" \
  "ds_read_b64_tr_b16 v[16:17], v10 offset:32768\n" \
  "ds_read_b64_tr_b16 v[18:19], v10 offset:34816\n" \
  "ds_read_b64_tr_b16 v[20:21], v10 offset:40960\n" \
  "ds_read_b64_tr_b16 v[22:23], v10 offset:43008\n" \
  "ds_read_b64_tr_b16 v[24:25], v10 offset:49152\n" \
  "ds_read_b64_tr_b16 v[26:27], v10 offset:51200\n" \
  "ds_read_b64_tr_b16 v[28:29], v10 offset:57344\n" \
  "ds_read_b64_tr_b16 v[30:31], v10 offset:59392\n" \
  "s_waitcnt lgkmcnt(4)\n" \
  "v_accvgpr_write_b32 a144, v16\n" \
  "v_accvgpr_write_b32 a145, v18\n" \
  "v_accvgpr_write_b32 a146, v17\n" \
  "v_accvgpr_write_b32 a147, v19\n" \
  "v_accvgpr_write_b32 a148, v20\n" \
  "v_accvgpr_write_b32 a149, v22\n" \
  "v_accvgpr_write_b32 a150, v21\n" \
  "v_accvgpr_write_b32 a151, v23\n" \
  "ds_read_b64_tr_b16 v[16:17], v11 offset:32768\n" \
  "ds_read_b64_tr_b16 v[18:19], v11 offset:34816\n" \
  "ds_read_b64_tr_b16 v[20:21], v11 offset:40960\n" \
  "ds_read_b64_tr_b16 v[22:23], v11 offset:43008\n" \
  "s_waitcnt lgkmcnt(4)\n" \
  "v_accvgpr_write_b32 a152, v24\n" \
  "v_accvgpr_write_b32 a153, v26\n" \
  "v_accvgpr_write_b32 a154, v25\n" \
  "v_accvgpr_write_b32 a155, v27\n" \
  "v_accvgpr_write_b32 a156, v28\n" \
  "v_accvgpr_write_b32 a157, v30\n" \
  "v_accvgpr_write_b32 a158, v29\n" \
  "v_accvgpr_write_b32 a159, v31\n" \
  "ds_read_b64_tr_b16 v[24:25], v11 offset:49152\n" \
  "ds_read_b64_tr_b16 v[26:27], v11 offset:51200\n" \
  "ds_read_b64_tr_b16 v[28:29], v11 offset:57344\n" \
  "ds_read_b64_tr_b16 v[30:31], v11 offset:59392\n" \
  "s_waitcnt lgkmcnt(4)\n" \
  "v_accvgpr_write_b32 a208, v16\n" \
  "v_accvgpr_write_b32 a209, v18\n" \
  "v_accvgpr_write_b32 a210, v17\n" \
  "v_accvgpr_write_b32 a211, v19\n" \
  "v_accvgpr_write_b32 a212, v20\n" \
  "v_accvgpr_write_b32 a213, v22\n" \
  "v_accvgpr_write_b32 a214, v21\n" \
  "v_accvgpr_write_b32 a215, v23\n" \
  "s_waitcnt lgkmcnt(0)\n" \
  "v_accvgpr_write_b32 a216, v24\n" \
  "v_accvgpr_write_b32 a217, v26\n" \
  "v_accvgpr_write_b32 a218, v25\n" \
  "v_accvgpr_write_b32 a219, v27\n" \
  "v_accvgpr_write_b32 a220, v28\n" \
  "v_accvgpr_write_b32 a221, v30\n" \
  "v_accvgpr_write_b32 a222, v29\n" \
  "v_accvgpr_write_b32 a223, v31\n" \
  "s_barrier\n" \
  "s_mov_b64 s[60:61], %1\n" \
  "s_and_b32 s61, s61, 0xffff\n" \
  "s_mov_b32 s62, 0x400000\n" \
  "s_mov_b32 s63, 0x20000\n" \
  "s_lshl_b32 s88, s77, 10\n" \
  "s_mul_i32 s89, s77, 32768\n" \
  "s_add_u32 m0, s88, 32768\n" \
  "s_add_u32 s90, s89, 1048576\n" \
  "buffer_load_dwordx4 v8, s[60:63], s90 offen lds\n" \
  "s_add_u32 m0, s88, 36864\n" \
  "s_add_u32 s90, s89, 1179648\n" \
  "buffer_load_dwordx4 v8, s[60:63], s90 offen lds\n" \
  "s_add_u32 m0, s88, 40960\n" \
  "s_add_u32 s90, s89, 1310720\n" \
  "buffer_load_dwordx4 v8, s[60:63], s90 offen lds\n" \
  "s_add_u32 m0, s88, 45056\n" \
  "s_add_u32 s90, s89, 1441792\n" \
  "buffer_load_dwordx4 v8, s[60:63], s90 offen lds\n" \
  "s_add_u32 m0, s88, 49152\n" \
  "s_add_u32 s90, s89, 1572864\n" \
  "buffer_load_dwordx4 v8, s[60:63], s90 offen lds\n" \
  "s_add_u32 m0, s88, 53248\n" \
  "s_add_u32 s90, s89, 1703936\n" \
  "buffer_load_dwordx4 v8, s[60:63], s90 offen lds\n" \
  "s_add_u32 m0, s88, 57344\n" \
  "s_add_u32 s90, s89, 1835008\n" \
  "buffer_load_dwordx4 v8, s[60:63], s90 offen lds\n" \
  "s_add_u32 m0, s88, 61440\n" \
  "s_add_u32 s90, s89, 1966080\n" \
  "buffer_load_dwordx4 v8, s[60:63], s90 offen lds\n" \
  "s_waitcnt vmcnt(24)\n" \
  "s_barrier\n" \
  "ds_read_b64_tr_b16 v[16:17], v12 offset:0\n" \
  "ds_read_b64_tr_b16 v[18:19], v12 offset:2048\n" \
  "ds_read_b64_tr_b16 v[20:21], v12 offset:8192\n" \
  "ds_read_b64_tr_b16 v[22:23], v12 offset:10240\n" \
  "ds_read_b64_tr_b16 v[24:25], v12 offset:16384\n" \
  "ds_read_b64_tr_b16 v[26:27], v12 offset:18432\n" \
  "ds_read_b64_tr_b16 v[28:29], v12 offset:24576\n" \
  "ds_read_b64_tr_b16 v[30:31], v12 offset:26624\n" \
  "s_waitcnt lgkmcnt(4)\n" \
  "v_accvgpr_write_b32 a160, v16\n" \
  "v_accvgpr_write_b32 a161, v18\n" \
  "v_accvgpr_write_b32 a162, v17\n" \
  "v_accvgpr_write_b32 a163, v19\n" \
  "v_accvgpr_write_b32 a164, v20\n" \
  "v_accvgpr_write_b32 a165, v22\n" \
  "v_accvgpr_write_b32 a166, v21\n" \
  "v_accvgpr_write_b32 a167, v23\n" \
  "ds_read_b64_tr_b16 v[16:17], v13 offset:0\n" \
  "ds_read_b64_tr_b16 v[18:19], v13 offset:2048\n" \
  "ds_read_b64_tr_b16 v[20:21], v13 offset:8192\n" \
  "ds_read_b64_tr_b16 v[22:23], v13 offset:10240\n" \
  "s_waitcnt lgkmcnt(4)\n" \
  "v_accvgpr_write_b32 a168, v24\n" \
  "v_accvgpr_write_b32 a169, v26\n" \
  "v_accvgpr_write_b32 a170, v25\n" \
  "v_accvgpr_write_b32 a171, v27\n" \
  "v_accvgpr_write_b32 a172, v28\n" \
  "v_accvgpr_write_b32 a173, v30\n" \
  "v_accvgpr_write_b32 a174, v29\n" \
  "v_accvgpr_write_b32 a175, v31\n" \
  "ds_read_b64_tr_b16 v[24:25], v13 offset:16384\n" \
  "ds_read_b64_tr_b16 v[26:27], v13 offset:18432\n" \
  "ds_read_b64_tr_b16 v[28:29], v13 offset:24576\n" \
  "ds_read_b64_tr_b16 v[30:31], v13 offset:26624\n" \
  "s_waitcnt lgkmcnt(4)\n" \
  "v_accvgpr_write_b32 a224, v16\n" \
  "v_accvgpr_write_b32 a225, v18\n" \
  "v_accvgpr_write_b32 a226, v17\n" \
  "v_accvgpr_write_b32 a227, v19\n" \
  "v_accvgpr_write_b32 a228, v20\n" \
  "v_accvgpr_write_b32 a229, v22\n" \
  "v_accvgpr_write_b32 a230, v21\n" \
  "v_accvgpr_write_b32 a231, v23\n" \
  "s_waitcnt lgkmcnt(0)\n" \
  "v_accvgpr_write_b32 a232, v24\n" \
  "v_accvgpr_write_b32 a233, v26\n" \
  "v_accvgpr_write_b32 a234, v25\n" \
  "v_accvgpr_write_b32 a235, v27\n" \
  "v_accvgpr_write_b32 a236, v28\n" \
  "v_accvgpr_write_b32 a237, v30\n" \
  "v_accvgpr_write_b32 a238, v29\n" \
  "v_accvgpr_write_b32 a239, v31\n" \
  "s_barrier\n" \
  "s_mov_b64 s[60:61], %1\n" \
  "s_and_b32 s61, s61, 0xffff\n" \
  "s_mov_b32 s62, 0x400000\n" \
  "s_mov_b32 s63, 0x20000\n" \
  "s_lshl_b32 s88, s77, 10\n" \
  "s_mul_i32 s89, s77, 32768\n" \
  "s_add_u32 m0, s88, 65536\n" \
  "s_add_u32 s90, s89, 2097152\n" \
  "buffer_load_dwordx4 v8, s[60:63], s90 offen lds\n" \
  "s_add_u32 m0, s88, 69632\n" \
  "s_add_u32 s90, s89, 2228224\n" \
  "buffer_load_dwordx4 v8, s[60:63], s90 offen lds\n" \
  "s_add_u32 m0, s88, 73728\n" \
  "s_add_u32 s90, s89, 2359296\n" \
  "buffer_load_dwordx4 v8, s[60:63], s90 offen lds\n" \
  "s_add_u32 m0, s88, 77824\n" \
  "s_add_u32 s90, s89, 2490368\n" \
  "buffer_load_dwordx4 v8, s[60:63], s90 offen lds\n" \
  "s_add_u32 m0, s88, 81920\n" \
  "s_add_u32 s90, s89, 2621440\n" \
  "buffer_load_dwordx4 v8, s[60:63], s90 offen lds\n" \
  "s_add_u32 m0, s88, 86016\n" \
  "s_add_u32 s90, s89, 2752512\n" \
  "buffer_load_dwordx4 v8, s[60:63], s90 offen lds\n" \
  "s_add_u32 m0, s88, 90112\n" \
  "s_add_u32 s90, s89, 2883584\n" \
  "buffer_load_dwordx4 v8, s[60:63], s90 offen lds\n" \
  "s_add_u32 m0, s88, 94208\n" \
  "s_add_u32 s90, s89, 3014656\n" \
  "buffer_load_dwordx4 v8, s[60:63], s90 offen lds\n" \
  "s_waitcnt vmcnt(24)\n" \
  "s_barrier\n" \
  "ds_read_b64_tr_b16 v[16:17], v12 offset:32768\n" \
  "ds_read_b64_tr_b16 v[18:19], v12 offset:34816\n" \
  "ds_read_b64_tr_b16 v[20:21], v12 offset:40960\n" \
  "ds_read_b64_tr_b16 v[22:23], v12 offset:43008\n" \
  "ds_read_b64_tr_b16 v[24:25], v12 offset:49152\n" \
  "ds_read_b64_tr_b16 v[26:27], v12 offset:51200\n" \
  "ds_read_b64_tr_b16 v[28:29], v12 offset:57344\n" \
  "ds_read_b64_tr_b16 v[30:31], v12 offset:59392\n" \
  "s_waitcnt lgkmcnt(4)\n" \
  "v_accvgpr_write_b32 a176, v16\n" \
  "v_accvgpr_write_b32 a177, v18\n" \
  "v_accvgpr_write_b32 a178, v17\n" \
  "v_accvgpr_write_b32 a179, v19\n" \
  "v_accvgpr_write_b32 a180, v20\n" \
  "v_accvgpr_write_b32 a181, v22\n" \
  "v_accvgpr_write_b32 a182, v21\n" \
  "v_accvgpr_write_b32 a183, v23\n" \
  "ds_read_b64_tr_b16 v[16:17], v13 offset:32768\n" \
  "ds_read_b64_tr_b16 v[18:19], v13 offset:34816\n" \
  "ds_read_b64_tr_b16 v[20:21], v13 offset:40960\n" \
  "ds_read_b64_tr_b16 v[22:23], v13 offset:43008\n" \
  "s_waitcnt lgkmcnt(4)\n" \
  "v_accvgpr_write_b32 a184, v24\n" \
  "v_accvgpr_write_b32 a185, v26\n" \
  "v_accvgpr_write_b32 a186, v25\n" \
  "v_accvgpr_write_b32 a187, v27\n" \
  "v_accvgpr_write_b32 a188, v28\n" \
  "v_accvgpr_write_b32 a189, v30\n" \
  "v_accvgpr_write_b32 a190, v29\n" \
  "v_accvgpr_write_b32 a191, v31\n" \
  "ds_read_b64_tr_b16 v[24:25], v13 offset:49152\n" \
  "ds_read_b64_tr_b16 v[26:27], v13 offset:51200\n" \
  "ds_read_b64_tr_b16 v[28:29], v13 offset:57344\n" \
  "ds_read_b64_tr_b16 v[30:31], v13 offset:59392\n" \
  "s_waitcnt lgkmcnt(4)\n" \
  "v_accvgpr_write_b32 a240, v16\n" \
  "v_accvgpr_write_b32 a241, v18\n" \
  "v_accvgpr_write_b32 a242, v17\n" \
  "v_accvgpr_write_b32 a243, v19\n" \
  "v_accvgpr_write_b32 a244, v20\n" \
  "v_accvgpr_write_b32 a245, v22\n" \
  "v_accvgpr_write_b32 a246, v21\n" \
  "v_accvgpr_write_b32 a247, v23\n" \
  "s_waitcnt lgkmcnt(0)\n" \
  "v_accvgpr_write_b32 a248, v24\n" \
  "v_accvgpr_write_b32 a249, v26\n" \
  "v_accvgpr_write_b32 a250, v25\n" \
  "v_accvgpr_write_b32 a251, v27\n" \
  "v_accvgpr_write_b32 a252, v28\n" \
  "v_accvgpr_write_b32 a253, v30\n" \
  "v_accvgpr_write_b32 a254, v29\n" \
  "v_accvgpr_write_b32 a255, v31\n" \
  "s_barrier\n" \
  "s_mov_b64 s[60:61], %1\n" \
  "s_and_b32 s61, s61, 0xffff\n" \
  "s_mov_b32 s62, 0x400000\n" \
  "s_mov_b32 s63, 0x20000\n" \
  "s_lshl_b32 s88, s77, 10\n" \
  "s_mul_i32 s89, s77, 32768\n" \
  "s_add_u32 m0, s88, 98304\n" \
  "s_add_u32 s90, s89, 3145728\n" \
  "buffer_load_dwordx4 v8, s[60:63], s90 offen lds\n" \
  "s_add_u32 m0, s88, 102400\n" \
  "s_add_u32 s90, s89, 3276800\n" \
  "buffer_load_dwordx4 v8, s[60:63], s90 offen lds\n" \
  "s_add_u32 m0, s88, 106496\n" \
  "s_add_u32 s90, s89, 3407872\n" \
  "buffer_load_dwordx4 v8, s[60:63], s90 offen lds\n" \
  "s_add_u32 m0, s88, 110592\n" \
  "s_add_u32 s90, s89, 3538944\n" \
  "buffer_load_dwordx4 v8, s[60:63], s90 offen lds\n" \
  "s_add_u32 m0, s88, 114688\n" \
  "s_add_u32 s90, s89, 3670016\n" \
  "buffer_load_dwordx4 v8, s[60:63], s90 offen lds\n" \
  "s_add_u32 m0, s88, 118784\n" \
  "s_add_u32 s90, s89, 3801088\n" \
  "buffer_load_dwordx4 v8, s[60:63], s90 offen lds\n" \
  "s_add_u32 m0, s88, 122880\n" \
  "s_add_u32 s90, s89, 3932160\n" \
  "buffer_load_dwordx4 v8, s[60:63], s90 offen lds\n" \
  "s_add_u32 m0, s88, 126976\n" \
  "s_add_u32 s90, s89, 4063232\n" \
  "buffer_load_dwordx4 v8, s[60:63], s90 offen lds\n" \
  "s_waitcnt vmcnt(24)\n" \
  "s_barrier\n" \
  "ds_read_b64_tr_b16 v[16:17], v10 offset:0\n" \
  "ds_read_b64_tr_b16 v[18:19], v10 offset:2048\n" \
  "ds_read_b64_tr_b16 v[20:21], v10 offset:8192\n" \
  "ds_read_b64_tr_b16 v[22:23], v10 offset:10240\n" \
  "ds_read_b64_tr_b16 v[24:25], v10 offset:16384\n" \
  "ds_read_b64_tr_b16 v[26:27], v10 offset:18432\n" \
  "ds_read_b64_tr_b16 v[28:29], v10 offset:24576\n" \
  "ds_read_b64_tr_b16 v[30:31], v10 offset:26624\n" \
  "s_waitcnt lgkmcnt(4)\n" \
  "v_accvgpr_write_b32 a0, v16\n" \
  "v_accvgpr_write_b32 a1, v18\n" \
  "v_accvgpr_write_b32 a2, v17\n" \
  "v_accvgpr_write_b32 a3, v19\n" \
  "v_accvgpr_write_b32 a4, v20\n" \
  "v_accvgpr_write_b32 a5, v22\n" \
  "v_accvgpr_write_b32 a6, v21\n" \
  "v_accvgpr_write_b32 a7, v23\n" \
  "ds_read_b64_tr_b16 v[16:17], v11 offset:0\n" \
  "ds_read_b64_tr_b16 v[18:19], v11 offset:2048\n" \
  "ds_read_b64_tr_b16 v[20:21], v11 offset:8192\n" \
  "ds_read_b64_tr_b16 v[22:23], v11 offset:10240\n" \
  "s_waitcnt lgkmcnt(4)\n" \
  "v_accvgpr_write_b32 a8, v24\n" \
  "v_accvgpr_write_b32 a9, v26\n" \
  "v_accvgpr_write_b32 a10, v25\n" \
  "v_accvgpr_write_b32 a11, v27\n" \
  "v_accvgpr_write_b32 a12, v28\n" \
  "v_accvgpr_write_b32 a13, v30\n" \
  "v_accvgpr_write_b32 a14, v29\n" \
  "v_accvgpr_write_b32 a15, v31\n" \
  "ds_read_b64_tr_b16 v[24:25], v11 offset:16384\n" \
  "ds_read_b64_tr_b16 v[26:27], v11 offset:18432\n" \
  "ds_read_b64_tr_b16 v[28:29], v11 offset:24576\n" \
  "ds_read_b64_tr_b16 v[30:31], v11 offset:26624\n" \
  "s_waitcnt lgkmcnt(4)\n" \
  "v_accvgpr_write_b32 a64, v16\n" \
  "v_accvgpr_write_b32 a65, v18\n" \
  "v_accvgpr_write_b32 a66, v17\n" \
  "v_accvgpr_write_b32 a67, v19\n" \
  "v_accvgpr_write_b32 a68, v20\n" \
  "v_accvgpr_write_b32 a69, v22\n" \
  "v_accvgpr_write_b32 a70, v21\n" \
  "v_accvgpr_write_b32 a71, v23\n" \
  "s_waitcnt lgkmcnt(0)\n" \
  "v_accvgpr_write_b32 a72, v24\n" \
  "v_accvgpr_write_b32 a73, v26\n" \
  "v_accvgpr_write_b32 a74, v25\n" \
  "v_accvgpr_write_b32 a75, v27\n" \
  "v_accvgpr_write_b32 a76, v28\n" \
  "v_accvgpr_write_b32 a77, v30\n" \
  "v_accvgpr_write_b32 a78, v29\n" \
  "v_accvgpr_write_b32 a79, v31\n" \
  "s_barrier\n" \
  "s_mov_b64 s[60:61], %3\n" \
  "s_and_b32 s61, s61, 0xffff\n" \
  "s_mov_b32 s62, 0x400000\n" \
  "s_mov_b32 s63, 0x20000\n" \
  "s_lshl_b32 s88, s77, 10\n" \
  "s_mul_i32 s89, s77, 32768\n" \
  "s_add_u32 m0, s88, 0\n" \
  "s_add_u32 s90, s89, 0\n" \
  "buffer_load_dwordx4 v8, s[60:63], s90 offen lds\n" \
  "s_add_u32 m0, s88, 4096\n" \
  "s_add_u32 s90, s89, 131072\n" \
  "buffer_load_dwordx4 v8, s[60:63], s90 offen lds\n" \
  "s_add_u32 m0, s88, 8192\n" \
  "s_add_u32 s90, s89, 262144\n" \
  "buffer_load_dwordx4 v8, s[60:63], s90 offen lds\n" \
  "s_add_u32 m0, s88, 12288\n" \
  "s_add_u32 s90, s89, 393216\n" \
  "buffer_load_dwordx4 v8, s[60:63], s90 offen lds\n" \
  "s_add_u32 m0, s88, 16384\n" \
  "s_add_u32 s90, s89, 524288\n" \
  "buffer_load_dwordx4 v8, s[60:63], s90 offen lds\n" \
  "s_add_u32 m0, s88, 20480\n" \
  "s_add_u32 s90, s89, 655360\n" \
  "buffer_load_dwordx4 v8, s[60:63], s90 offen lds\n" \
  "s_add_u32 m0, s88, 24576\n" \
  "s_add_u32 s90, s89, 786432\n" \
  "buffer_load_dwordx4 v8, s[60:63], s90 offen lds\n" \
  "s_add_u32 m0, s88, 28672\n" \
  "s_add_u32 s90, s89, 917504\n" \
  "buffer_load_dwordx4 v8, s[60:63], s90 offen lds\n" \
  "s_waitcnt vmcnt(24)\n" \
  "s_barrier\n" \
  "ds_read_b64_tr_b16 v[16:17], v10 offset:32768\n" \
  "ds_read_b64_tr_b16 v[18:19], v10 offset:34816\n" \
  "ds_read_b64_tr_b16 v[20:21], v10 offset:40960\n" \
  "ds_read_b64_tr_b16 v[22:23], v10 offset:43008\n" \
  "ds_read_b64_tr_b16 v[24:25], v10 offset:49152\n" \
  "ds_read_b64_tr_b16 v[26:27], v10 offset:51200\n" \
  "ds_read_b64_tr_b16 v[28:29], v10 offset:57344\n" \
  "ds_read_b64_tr_b16 v[30:31], v10 offset:59392\n" \
  "s_waitcnt lgkmcnt(4)\n" \
  "v_accvgpr_write_b32 a16, v16\n" \
  "v_accvgpr_write_b32 a17, v18\n" \
  "v_accvgpr_write_b32 a18, v17\n" \
  "v_accvgpr_write_b32 a19, v19\n" \
  "v_accvgpr_write_b32 a20, v20\n" \
  "v_accvgpr_write_b32 a21, v22\n" \
  "v_accvgpr_write_b32 a22, v21\n" \
  "v_accvgpr_write_b32 a23, v23\n" \
  "ds_read_b64_tr_b16 v[16:17], v11 offset:32768\n" \
  "ds_read_b64_tr_b16 v[18:19], v11 offset:34816\n" \
  "ds_read_b64_tr_b16 v[20:21], v11 offset:40960\n" \
  "ds_read_b64_tr_b16 v[22:23], v11 offset:43008\n" \
  "s_waitcnt lgkmcnt(4)\n" \
  "v_accvgpr_write_b32 a24, v24\n" \
  "v_accvgpr_write_b32 a25, v26\n" \
  "v_accvgpr_write_b32 a26, v25\n" \
  "v_accvgpr_write_b32 a27, v27\n" \
  "v_accvgpr_write_b32 a28, v28\n" \
  "v_accvgpr_write_b32 a29, v30\n" \
  "v_accvgpr_write_b32 a30, v29\n" \
  "v_accvgpr_write_b32 a31, v31\n" \
  "ds_read_b64_tr_b16 v[24:25], v11 offset:49152\n" \
  "ds_read_b64_tr_b16 v[26:27], v11 offset:51200\n" \
  "ds_read_b64_tr_b16 v[28:29], v11 offset:57344\n" \
  "ds_read_b64_tr_b16 v[30:31], v11 offset:59392\n" \
  "s_waitcnt lgkmcnt(4)\n" \
  "v_accvgpr_write_b32 a80, v16\n" \
  "v_accvgpr_write_b32 a81, v18\n" \
  "v_accvgpr_write_b32 a82, v17\n" \
  "v_accvgpr_write_b32 a83, v19\n" \
  "v_accvgpr_write_b32 a84, v20\n" \
  "v_accvgpr_write_b32 a85, v22\n" \
  "v_accvgpr_write_b32 a86, v21\n" \
  "v_accvgpr_write_b32 a87, v23\n" \
  "s_waitcnt lgkmcnt(0)\n" \
  "v_accvgpr_write_b32 a88, v24\n" \
  "v_accvgpr_write_b32 a89, v26\n" \
  "v_accvgpr_write_b32 a90, v25\n" \
  "v_accvgpr_write_b32 a91, v27\n" \
  "v_accvgpr_write_b32 a92, v28\n" \
  "v_accvgpr_write_b32 a93, v30\n" \
  "v_accvgpr_write_b32 a94, v29\n" \
  "v_accvgpr_write_b32 a95, v31\n" \
  "s_barrier\n" \
  "s_mov_b64 s[60:61], %3\n" \
  "s_and_b32 s61, s61, 0xffff\n" \
  "s_mov_b32 s62, 0x400000\n" \
  "s_mov_b32 s63, 0x20000\n" \
  "s_lshl_b32 s88, s77, 10\n" \
  "s_mul_i32 s89, s77, 32768\n" \
  "s_add_u32 m0, s88, 32768\n" \
  "s_add_u32 s90, s89, 1048576\n" \
  "buffer_load_dwordx4 v8, s[60:63], s90 offen lds\n" \
  "s_add_u32 m0, s88, 36864\n" \
  "s_add_u32 s90, s89, 1179648\n" \
  "buffer_load_dwordx4 v8, s[60:63], s90 offen lds\n" \
  "s_add_u32 m0, s88, 40960\n" \
  "s_add_u32 s90, s89, 1310720\n" \
  "buffer_load_dwordx4 v8, s[60:63], s90 offen lds\n" \
  "s_add_u32 m0, s88, 45056\n" \
  "s_add_u32 s90, s89, 1441792\n" \
  "buffer_load_dwordx4 v8, s[60:63], s90 offen lds\n" \
  "s_add_u32 m0, s88, 49152\n" \
  "s_add_u32 s90, s89, 1572864\n" \
  "buffer_load_dwordx4 v8, s[60:63], s90 offen lds\n" \
  "s_add_u32 m0, s88, 53248\n" \
  "s_add_u32 s90, s89, 1703936\n" \
  "buffer_load_dwordx4 v8, s[60:63], s90 offen lds\n" \
  "s_add_u32 m0, s88, 57344\n" \
  "s_add_u32 s90, s89, 1835008\n" \
  "buffer_load_dwordx4 v8, s[60:63], s90 offen lds\n" \
  "s_add_u32 m0, s88, 61440\n" \
  "s_add_u32 s90, s89, 1966080\n" \
  "buffer_load_dwordx4 v8, s[60:63], s90 offen lds\n" \
  "s_waitcnt vmcnt(24)\n" \
  "s_barrier\n" \
  "ds_read_b64_tr_b16 v[16:17], v12 offset:0\n" \
  "ds_read_b64_tr_b16 v[18:19], v12 offset:2048\n" \
  "ds_read_b64_tr_b16 v[20:21], v12 offset:8192\n" \
  "ds_read_b64_tr_b16 v[22:23], v12 offset:10240\n" \
  "ds_read_b64_tr_b16 v[24:25], v12 offset:16384\n" \
  "ds_read_b64_tr_b16 v[26:27], v12 offset:18432\n" \
  "ds_read_b64_tr_b16 v[28:29], v12 offset:24576\n" \
  "ds_read_b64_tr_b16 v[30:31], v12 offset:26624\n" \
  "s_waitcnt lgkmcnt(4)\n" \
  "v_accvgpr_write_b32 a32, v16\n" \
  "v_accvgpr_write_b32 a33, v18\n" \
  "v_accvgpr_write_b32 a34, v17\n" \
  "v_accvgpr_write_b32 a35, v19\n" \
  "v_accvgpr_write_b32 a36, v20\n" \
  "v_accvgpr_write_b32 a37, v22\n" \
  "v_accvgpr_write_b32 a38, v21\n" \
  "v_accvgpr_write_b32 a39, v23\n" \
  "ds_read_b64_tr_b16 v[16:17], v13 offset:0\n" \
  "ds_read_b64_tr_b16 v[18:19], v13 offset:2048\n" \
  "ds_read_b64_tr_b16 v[20:21], v13 offset:8192\n" \
  "ds_read_b64_tr_b16 v[22:23], v13 offset:10240\n" \
  "s_waitcnt lgkmcnt(4)\n" \
  "v_accvgpr_write_b32 a40, v24\n" \
  "v_accvgpr_write_b32 a41, v26\n" \
  "v_accvgpr_write_b32 a42, v25\n" \
  "v_accvgpr_write_b32 a43, v27\n" \
  "v_accvgpr_write_b32 a44, v28\n" \
  "v_accvgpr_write_b32 a45, v30\n" \
  "v_accvgpr_write_b32 a46, v29\n" \
  "v_accvgpr_write_b32 a47, v31\n" \
  "ds_read_b64_tr_b16 v[24:25], v13 offset:16384\n" \
  "ds_read_b64_tr_b16 v[26:27], v13 offset:18432\n" \
  "ds_read_b64_tr_b16 v[28:29], v13 offset:24576\n" \
  "ds_read_b64_tr_b16 v[30:31], v13 offset:26624\n" \
  "s_waitcnt lgkmcnt(4)\n" \
  "v_accvgpr_write_b32 a96, v16\n" \
  "v_accvgpr_write_b32 a97, v18\n" \
  "v_accvgpr_write_b32 a98, v17\n" \
  "v_accvgpr_write_b32 a99, v19\n" \
  "v_accvgpr_write_b32 a100, v20\n" \
  "v_accvgpr_write_b32 a101, v22\n" \
  "v_accvgpr_write_b32 a102, v21\n" \
  "v_accvgpr_write_b32 a103, v23\n" \
  "s_waitcnt lgkmcnt(0)\n" \
  "v_accvgpr_write_b32 a104, v24\n" \
  "v_accvgpr_write_b32 a105, v26\n" \
  "v_accvgpr_write_b32 a106, v25\n" \
  "v_accvgpr_write_b32 a107, v27\n" \
  "v_accvgpr_write_b32 a108, v28\n" \
  "v_accvgpr_write_b32 a109, v30\n" \
  "v_accvgpr_write_b32 a110, v29\n" \
  "v_accvgpr_write_b32 a111, v31\n" \
  "s_barrier\n" \
  "s_mov_b64 s[60:61], %3\n" \
  "s_and_b32 s61, s61, 0xffff\n" \
  "s_mov_b32 s62, 0x400000\n" \
  "s_mov_b32 s63, 0x20000\n" \
  "s_lshl_b32 s88, s77, 10\n" \
  "s_mul_i32 s89, s77, 32768\n" \
  "s_add_u32 m0, s88, 65536\n" \
  "s_add_u32 s90, s89, 2097152\n" \
  "buffer_load_dwordx4 v8, s[60:63], s90 offen lds\n" \
  "s_add_u32 m0, s88, 69632\n" \
  "s_add_u32 s90, s89, 2228224\n" \
  "buffer_load_dwordx4 v8, s[60:63], s90 offen lds\n" \
  "s_add_u32 m0, s88, 73728\n" \
  "s_add_u32 s90, s89, 2359296\n" \
  "buffer_load_dwordx4 v8, s[60:63], s90 offen lds\n" \
  "s_add_u32 m0, s88, 77824\n" \
  "s_add_u32 s90, s89, 2490368\n" \
  "buffer_load_dwordx4 v8, s[60:63], s90 offen lds\n" \
  "s_add_u32 m0, s88, 81920\n" \
  "s_add_u32 s90, s89, 2621440\n" \
  "buffer_load_dwordx4 v8, s[60:63], s90 offen lds\n" \
  "s_add_u32 m0, s88, 86016\n" \
  "s_add_u32 s90, s89, 2752512\n" \
  "buffer_load_dwordx4 v8, s[60:63], s90 offen lds\n" \
  "s_add_u32 m0, s88, 90112\n" \
  "s_add_u32 s90, s89, 2883584\n" \
  "buffer_load_dwordx4 v8, s[60:63], s90 offen lds\n" \
  "s_add_u32 m0, s88, 94208\n" \
  "s_add_u32 s90, s89, 3014656\n" \
  "buffer_load_dwordx4 v8, s[60:63], s90 offen lds\n" \
  "s_waitcnt vmcnt(24)\n" \
  "s_barrier\n" \
  "ds_read_b64_tr_b16 v[16:17], v12 offset:32768\n" \
  "ds_read_b64_tr_b16 v[18:19], v12 offset:34816\n" \
  "ds_read_b64_tr_b16 v[20:21], v12 offset:40960\n" \
  "ds_read_b64_tr_b16 v[22:23], v12 offset:43008\n" \
  "ds_read_b64_tr_b16 v[24:25], v12 offset:49152\n" \
  "ds_read_b64_tr_b16 v[26:27], v12 offset:51200\n" \
  "ds_read_b64_tr_b16 v[28:29], v12 offset:57344\n" \
  "ds_read_b64_tr_b16 v[30:31], v12 offset:59392\n" \
  "s_waitcnt lgkmcnt(4)\n" \
  "v_accvgpr_write_b32 a48, v16\n" \
  "v_accvgpr_write_b32 a49, v18\n" \
  "v_accvgpr_write_b32 a50, v17\n" \
  "v_accvgpr_write_b32 a51, v19\n" \
  "v_accvgpr_write_b32 a52, v20\n" \
  "v_accvgpr_write_b32 a53, v22\n" \
  "v_accvgpr_write_b32 a54, v21\n" \
  "v_accvgpr_write_b32 a55, v23\n" \
  "ds_read_b64_tr_b16 v[16:17], v13 offset:32768\n" \
  "ds_read_b64_tr_b16 v[18:19], v13 offset:34816\n" \
  "ds_read_b64_tr_b16 v[20:21], v13 offset:40960\n" \
  "ds_read_b64_tr_b16 v[22:23], v13 offset:43008\n" \
  "s_waitcnt lgkmcnt(4)\n" \
  "v_accvgpr_write_b32 a56, v24\n" \
  "v_accvgpr_write_b32 a57, v26\n" \
  "v_accvgpr_write_b32 a58, v25\n" \
  "v_accvgpr_write_b32 a59, v27\n" \
  "v_accvgpr_write_b32 a60, v28\n" \
  "v_accvgpr_write_b32 a61, v30\n" \
  "v_accvgpr_write_b32 a62, v29\n" \
  "v_accvgpr_write_b32 a63, v31\n" \
  "ds_read_b64_tr_b16 v[24:25], v13 offset:49152\n" \
  "ds_read_b64_tr_b16 v[26:27], v13 offset:51200\n" \
  "ds_read_b64_tr_b16 v[28:29], v13 offset:57344\n" \
  "ds_read_b64_tr_b16 v[30:31], v13 offset:59392\n" \
  "s_waitcnt lgkmcnt(4)\n" \
  "v_accvgpr_write_b32 a112, v16\n" \
  "v_accvgpr_write_b32 a113, v18\n" \
  "v_accvgpr_write_b32 a114, v17\n" \
  "v_accvgpr_write_b32 a115, v19\n" \
  "v_accvgpr_write_b32 a116, v20\n" \
  "v_accvgpr_write_b32 a117, v22\n" \
  "v_accvgpr_write_b32 a118, v21\n" \
  "v_accvgpr_write_b32 a119, v23\n" \
  "s_waitcnt lgkmcnt(0)\n" \
  "v_accvgpr_write_b32 a120, v24\n" \
  "v_accvgpr_write_b32 a121, v26\n" \
  "v_accvgpr_write_b32 a122, v25\n" \
  "v_accvgpr_write_b32 a123, v27\n" \
  "v_accvgpr_write_b32 a124, v28\n" \
  "v_accvgpr_write_b32 a125, v30\n" \
  "v_accvgpr_write_b32 a126, v29\n" \
  "v_accvgpr_write_b32 a127, v31\n" \
  "s_barrier\n" \
  "s_mov_b64 s[60:61], %3\n" \
  "s_and_b32 s61, s61, 0xffff\n" \
  "s_mov_b32 s62, 0x400000\n" \
  "s_mov_b32 s63, 0x20000\n" \
  "s_lshl_b32 s88, s77, 10\n" \
  "s_mul_i32 s89, s77, 32768\n" \
  "s_add_u32 m0, s88, 98304\n" \
  "s_add_u32 s90, s89, 3145728\n" \
  "buffer_load_dwordx4 v8, s[60:63], s90 offen lds\n" \
  "s_add_u32 m0, s88, 102400\n" \
  "s_add_u32 s90, s89, 3276800\n" \
  "buffer_load_dwordx4 v8, s[60:63], s90 offen lds\n" \
  "s_add_u32 m0, s88, 106496\n" \
  "s_add_u32 s90, s89, 3407872\n" \
  "buffer_load_dwordx4 v8, s[60:63], s90 offen lds\n" \
  "s_add_u32 m0, s88, 110592\n" \
  "s_add_u32 s90, s89, 3538944\n" \
  "buffer_load_dwordx4 v8, s[60:63], s90 offen lds\n" \
  "s_add_u32 m0, s88, 114688\n" \
  "s_add_u32 s90, s89, 3670016\n" \
  "buffer_load_dwordx4 v8, s[60:63], s90 offen lds\n" \
  "s_add_u32 m0, s88, 118784\n" \
  "s_add_u32 s90, s89, 3801088\n" \
  "buffer_load_dwordx4 v8, s[60:63], s90 offen lds\n" \
  "s_add_u32 m0, s88, 122880\n" \
  "s_add_u32 s90, s89, 3932160\n" \
  "buffer_load_dwordx4 v8, s[60:63], s90 offen lds\n" \
  "s_add_u32 m0, s88, 126976\n" \
  "s_add_u32 s90, s89, 4063232\n" \
  "buffer_load_dwordx4 v8, s[60:63], s90 offen lds\n" \
  "s_waitcnt vmcnt(24)\n" \
  "s_barrier\n" \
  "ds_read_b64_tr_b16 v[16:17], v10 offset:0\n" \
  "ds_read_b64_tr_b16 v[18:19], v10 offset:2048\n" \
  "ds_read_b64_tr_b16 v[20:21], v10 offset:8192\n" \
  "ds_read_b64_tr_b16 v[22:23], v10 offset:10240\n" \
  "ds_read_b64_tr_b16 v[24:25], v10 offset:16384\n" \
  "ds_read_b64_tr_b16 v[26:27], v10 offset:18432\n" \
  "ds_read_b64_tr_b16 v[28:29], v10 offset:24576\n" \
  "ds_read_b64_tr_b16 v[30:31], v10 offset:26624\n" \
  "s_waitcnt lgkmcnt(4)\n" \
  "v_mov_b32 v128, v16\n" \
  "v_mov_b32 v129, v18\n" \
  "v_mov_b32 v130, v17\n" \
  "v_mov_b32 v131, v19\n" \
  "v_mov_b32 v132, v20\n" \
  "v_mov_b32 v133, v22\n" \
  "v_mov_b32 v134, v21\n" \
  "v_mov_b32 v135, v23\n" \
  "ds_read_b64_tr_b16 v[16:17], v11 offset:0\n" \
  "ds_read_b64_tr_b16 v[18:19], v11 offset:2048\n" \
  "ds_read_b64_tr_b16 v[20:21], v11 offset:8192\n" \
  "ds_read_b64_tr_b16 v[22:23], v11 offset:10240\n" \
  "s_waitcnt lgkmcnt(4)\n" \
  "v_mov_b32 v136, v24\n" \
  "v_mov_b32 v137, v26\n" \
  "v_mov_b32 v138, v25\n" \
  "v_mov_b32 v139, v27\n" \
  "v_mov_b32 v140, v28\n" \
  "v_mov_b32 v141, v30\n" \
  "v_mov_b32 v142, v29\n" \
  "v_mov_b32 v143, v31\n" \
  "ds_read_b64_tr_b16 v[24:25], v11 offset:16384\n" \
  "ds_read_b64_tr_b16 v[26:27], v11 offset:18432\n" \
  "ds_read_b64_tr_b16 v[28:29], v11 offset:24576\n" \
  "ds_read_b64_tr_b16 v[30:31], v11 offset:26624\n" \
  "s_waitcnt lgkmcnt(4)\n" \
  "v_mov_b32 v192, v16\n" \
  "v_mov_b32 v193, v18\n" \
  "v_mov_b32 v194, v17\n" \
  "v_mov_b32 v195, v19\n" \
  "v_mov_b32 v196, v20\n" \
  "v_mov_b32 v197, v22\n" \
  "v_mov_b32 v198, v21\n" \
  "v_mov_b32 v199, v23\n" \
  "s_waitcnt lgkmcnt(0)\n" \
  "v_mov_b32 v200, v24\n" \
  "v_mov_b32 v201, v26\n" \
  "v_mov_b32 v202, v25\n" \
  "v_mov_b32 v203, v27\n" \
  "v_mov_b32 v204, v28\n" \
  "v_mov_b32 v205, v30\n" \
  "v_mov_b32 v206, v29\n" \
  "v_mov_b32 v207, v31\n" \
  "s_barrier\n" \
  "s_mov_b64 s[60:61], %4\n" \
  "s_and_b32 s61, s61, 0xffff\n" \
  "s_mov_b32 s62, 0x400000\n" \
  "s_mov_b32 s63, 0x20000\n" \
  "s_lshl_b32 s88, s77, 10\n" \
  "s_mul_i32 s89, s77, 4096\n" \
  "s_add_u32 m0, s88, 0\n" \
  "s_add_u32 s90, s89, 0\n" \
  "buffer_load_dwordx4 v9, s[60:63], s90 offen lds\n" \
  "s_add_u32 m0, s88, 4096\n" \
  "s_add_u32 s90, s89, 16384\n" \
  "buffer_load_dwordx4 v9, s[60:63], s90 offen lds\n" \
  "s_add_u32 m0, s88, 8192\n" \
  "s_add_u32 s90, s89, 32768\n" \
  "buffer_load_dwordx4 v9, s[60:63], s90 offen lds\n" \
  "s_add_u32 m0, s88, 12288\n" \
  "s_add_u32 s90, s89, 49152\n" \
  "buffer_load_dwordx4 v9, s[60:63], s90 offen lds\n" \
  "s_add_u32 m0, s88, 16384\n" \
  "s_add_u32 s90, s89, 65536\n" \
  "buffer_load_dwordx4 v9, s[60:63], s90 offen lds\n" \
  "s_add_u32 m0, s88, 20480\n" \
  "s_add_u32 s90, s89, 81920\n" \
  "buffer_load_dwordx4 v9, s[60:63], s90 offen lds\n" \
  "s_add_u32 m0, s88, 24576\n" \
  "s_add_u32 s90, s89, 98304\n" \
  "buffer_load_dwordx4 v9, s[60:63], s90 offen lds\n" \
  "s_add_u32 m0, s88, 28672\n" \
  "s_add_u32 s90, s89, 114688\n" \
  "buffer_load_dwordx4 v9, s[60:63], s90 offen lds\n" \
  "s_waitcnt vmcnt(24)\n" \
  "s_barrier\n" \
  "ds_read_b64_tr_b16 v[16:17], v10 offset:32768\n" \
  "ds_read_b64_tr_b16 v[18:19], v10 offset:34816\n" \
  "ds_read_b64_tr_b16 v[20:21], v10 offset:40960\n" \
  "ds_read_b64_tr_b16 v[22:23], v10 offset:43008\n" \
  "ds_read_b64_tr_b16 v[24:25], v10 offset:49152\n" \
  "ds_read_b64_tr_b16 v[26:27], v10 offset:51200\n" \
  "ds_read_b64_tr_b16 v[28:29], v10 offset:57344\n" \
  "ds_read_b64_tr_b16 v[30:31], v10 offset:59392\n" \
  "s_waitcnt lgkmcnt(4)\n" \
  "v_mov_b32 v144, v16\n" \
  "v_mov_b32 v145, v18\n" \
  "v_mov_b32 v146, v17\n" \
  "v_mov_b32 v147, v19\n" \
  "v_mov_b32 v148, v20\n" \
  "v_mov_b32 v149, v22\n" \
  "v_mov_b32 v150, v21\n" \
  "v_mov_b32 v151, v23\n" \
  "ds_read_b64_tr_b16 v[16:17], v11 offset:32768\n" \
  "ds_read_b64_tr_b16 v[18:19], v11 offset:34816\n" \
  "ds_read_b64_tr_b16 v[20:21], v11 offset:40960\n" \
  "ds_read_b64_tr_b16 v[22:23], v11 offset:43008\n" \
  "s_waitcnt lgkmcnt(4)\n" \
  "v_mov_b32 v152, v24\n" \
  "v_mov_b32 v153, v26\n" \
  "v_mov_b32 v154, v25\n" \
  "v_mov_b32 v155, v27\n" \
  "v_mov_b32 v156, v28\n" \
  "v_mov_b32 v157, v30\n" \
  "v_mov_b32 v158, v29\n" \
  "v_mov_b32 v159, v31\n" \
  "ds_read_b64_tr_b16 v[24:25], v11 offset:49152\n" \
  "ds_read_b64_tr_b16 v[26:27], v11 offset:51200\n" \
  "ds_read_b64_tr_b16 v[28:29], v11 offset:57344\n" \
  "ds_read_b64_tr_b16 v[30:31], v11 offset:59392\n" \
  "s_waitcnt lgkmcnt(4)\n" \
  "v_mov_b32 v208, v16\n" \
  "v_mov_b32 v209, v18\n" \
  "v_mov_b32 v210, v17\n" \
  "v_mov_b32 v211, v19\n" \
  "v_mov_b32 v212, v20\n" \
  "v_mov_b32 v213, v22\n" \
  "v_mov_b32 v214, v21\n" \
  "v_mov_b32 v215, v23\n" \
  "s_waitcnt lgkmcnt(0)\n" \
  "v_mov_b32 v216, v24\n" \
  "v_mov_b32 v217, v26\n" \
  "v_mov_b32 v218, v25\n" \
  "v_mov_b32 v219, v27\n" \
  "v_mov_b32 v220, v28\n" \
  "v_mov_b32 v221, v30\n" \
  "v_mov_b32 v222, v29\n" \
  "v_mov_b32 v223, v31\n" \
  "s_barrier\n" \
  "s_waitcnt vmcnt(16)\n" \
  "s_barrier\n" \
  "ds_read_b64_tr_b16 v[16:17], v12 offset:0\n" \
  "ds_read_b64_tr_b16 v[18:19], v12 offset:2048\n" \
  "ds_read_b64_tr_b16 v[20:21], v12 offset:8192\n" \
  "ds_read_b64_tr_b16 v[22:23], v12 offset:10240\n" \
  "ds_read_b64_tr_b16 v[24:25], v12 offset:16384\n" \
  "ds_read_b64_tr_b16 v[26:27], v12 offset:18432\n" \
  "ds_read_b64_tr_b16 v[28:29], v12 offset:24576\n" \
  "ds_read_b64_tr_b16 v[30:31], v12 offset:26624\n" \
  "s_waitcnt lgkmcnt(4)\n" \
  "v_mov_b32 v160, v16\n" \
  "v_mov_b32 v161, v18\n" \
  "v_mov_b32 v162, v17\n" \
  "v_mov_b32 v163, v19\n" \
  "v_mov_b32 v164, v20\n" \
  "v_mov_b32 v165, v22\n" \
  "v_mov_b32 v166, v21\n" \
  "v_mov_b32 v167, v23\n" \
  "ds_read_b64_tr_b16 v[16:17], v13 offset:0\n" \
  "ds_read_b64_tr_b16 v[18:19], v13 offset:2048\n" \
  "ds_read_b64_tr_b16 v[20:21], v13 offset:8192\n" \
  "ds_read_b64_tr_b16 v[22:23], v13 offset:10240\n" \
  "s_waitcnt lgkmcnt(4)\n" \
  "v_mov_b32 v168, v24\n" \
  "v_mov_b32 v169, v26\n" \
  "v_mov_b32 v170, v25\n" \
  "v_mov_b32 v171, v27\n" \
  "v_mov_b32 v172, v28\n" \
  "v_mov_b32 v173, v30\n" \
  "v_mov_b32 v174, v29\n" \
  "v_mov_b32 v175, v31\n" \
  "ds_read_b64_tr_b16 v[24:25], v13 offset:16384\n" \
  "ds_read_b64_tr_b16 v[26:27], v13 offset:18432\n" \
  "ds_read_b64_tr_b16 v[28:29], v13 offset:24576\n" \
  "ds_read_b64_tr_b16 v[30:31], v13 offset:26624\n" \
  "s_waitcnt lgkmcnt(4)\n" \
  "v_mov_b32 v224, v16\n" \
  "v_mov_b32 v225, v18\n" \
  "v_mov_b32 v226, v17\n" \
  "v_mov_b32 v227, v19\n" \
  "v_mov_b32 v228, v20\n" \
  "v_mov_b32 v229, v22\n" \
  "v_mov_b32 v230, v21\n" \
  "v_mov_b32 v231, v23\n" \
  "s_waitcnt lgkmcnt(0)\n" \
  "v_mov_b32 v232, v24\n" \
  "v_mov_b32 v233, v26\n" \
  "v_mov_b32 v234, v25\n" \
  "v_mov_b32 v235, v27\n" \
  "v_mov_b32 v236, v28\n" \
  "v_mov_b32 v237, v30\n" \
  "v_mov_b32 v238, v29\n" \
  "v_mov_b32 v239, v31\n" \
  "s_barrier\n" \
  "s_waitcnt vmcnt(8)\n" \
  "s_barrier\n" \
  "ds_read_b64_tr_b16 v[16:17], v12 offset:32768\n" \
  "ds_read_b64_tr_b16 v[18:19], v12 offset:34816\n" \
  "ds_read_b64_tr_b16 v[20:21], v12 offset:40960\n" \
  "ds_read_b64_tr_b16 v[22:23], v12 offset:43008\n" \
  "ds_read_b64_tr_b16 v[24:25], v12 offset:49152\n" \
  "ds_read_b64_tr_b16 v[26:27], v12 offset:51200\n" \
  "ds_read_b64_tr_b16 v[28:29], v12 offset:57344\n" \
  "ds_read_b64_tr_b16 v[30:31], v12 offset:59392\n" \
  "s_waitcnt lgkmcnt(4)\n" \
  "v_mov_b32 v176, v16\n" \
  "v_mov_b32 v177, v18\n" \
  "v_mov_b32 v178, v17\n" \
  "v_mov_b32 v179, v19\n" \
  "v_mov_b32 v180, v20\n" \
  "v_mov_b32 v181, v22\n" \
  "v_mov_b32 v182, v21\n" \
  "v_mov_b32 v183, v23\n" \
  "ds_read_b64_tr_b16 v[16:17], v13 offset:32768\n" \
  "ds_read_b64_tr_b16 v[18:19], v13 offset:34816\n" \
  "ds_read_b64_tr_b16 v[20:21], v13 offset:40960\n" \
  "ds_read_b64_tr_b16 v[22:23], v13 offset:43008\n" \
  "s_waitcnt lgkmcnt(4)\n" \
  "v_mov_b32 v184, v24\n" \
  "v_mov_b32 v185, v26\n" \
  "v_mov_b32 v186, v25\n" \
  "v_mov_b32 v187, v27\n" \
  "v_mov_b32 v188, v28\n" \
  "v_mov_b32 v189, v30\n" \
  "v_mov_b32 v190, v29\n" \
  "v_mov_b32 v191, v31\n" \
  "ds_read_b64_tr_b16 v[24:25], v13 offset:49152\n" \
  "ds_read_b64_tr_b16 v[26:27], v13 offset:51200\n" \
  "ds_read_b64_tr_b16 v[28:29], v13 offset:57344\n" \
  "ds_read_b64_tr_b16 v[30:31], v13 offset:59392\n" \
  "s_waitcnt lgkmcnt(4)\n" \
  "v_mov_b32 v240, v16\n" \
  "v_mov_b32 v241, v18\n" \
  "v_mov_b32 v242, v17\n" \
  "v_mov_b32 v243, v19\n" \
  "v_mov_b32 v244, v20\n" \
  "v_mov_b32 v245, v22\n" \
  "v_mov_b32 v246, v21\n" \
  "v_mov_b32 v247, v23\n" \
  "s_waitcnt lgkmcnt(0)\n" \
  "v_mov_b32 v248, v24\n" \
  "v_mov_b32 v249, v26\n" \
  "v_mov_b32 v250, v25\n" \
  "v_mov_b32 v251, v27\n" \
  "v_mov_b32 v252, v28\n" \
  "v_mov_b32 v253, v30\n" \
  "v_mov_b32 v254, v29\n" \
  "v_mov_b32 v255, v31\n" \
  "s_barrier\n" \
  "s_waitcnt vmcnt(0)\n" \
  "s_barrier\n" \
  "ds_read_b64_tr_b16 v[96:97], v14 offset:0\n" \
  "ds_read_b64_tr_b16 v[98:99], v14 offset:4096\n" \
  "ds_read_b64_tr_b16 v[100:101], v14 offset:8192\n" \
  "ds_read_b64_tr_b16 v[102:103], v14 offset:12288\n" \
  "ds_read_b64_tr_b16 v[104:105], v14 offset:16384\n" \
  "ds_read_b64_tr_b16 v[106:107], v14 offset:20480\n" \
  "ds_read_b64_tr_b16 v[108:109], v14 offset:24576\n" \
  "ds_read_b64_tr_b16 v[110:111], v14 offset:28672\n" \
  "s_waitcnt lgkmcnt(0)\n" \
  "ds_read_b64_tr_b16 v[112:113], v15 offset:0\n" \
  "ds_read_b64_tr_b16 v[114:115], v15 offset:4096\n" \
  "ds_read_b64_tr_b16 v[116:117], v15 offset:8192\n" \
  "ds_read_b64_tr_b16 v[118:119], v15 offset:12288\n" \
  "ds_read_b64_tr_b16 v[120:121], v15 offset:16384\n" \
  "ds_read_b64_tr_b16 v[122:123], v15 offset:20480\n" \
  "ds_read_b64_tr_b16 v[124:125], v15 offset:24576\n" \
  "ds_read_b64_tr_b16 v[126:127], v15 offset:28672\n" \
  "s_waitcnt lgkmcnt(0)\n" \
  "s_barrier\n" \
  "v_lshrrev_b32 v4, 5, v95\n" \
  "v_lshl_add_u32 v4, v4, 4, v1\n" \
  "s_lshl_b32 s88, s77, 5\n" \
  "v_add_u32 v4, s88, v4\n" \
  "v_lshlrev_b32 v4, 4, v4\n" \
  "v_and_b32 v5, 1, v0\n" \
  "v_lshl_add_u32 v34, v5, 3, v4\n" \
  "v_lshl_add_u32 v4, v0, 2, v2\n" \
  "v_lshlrev_b32 v4, 4, v4\n" \
  "v_and_b32 v5, 1, v3\n" \
  "v_lshl_add_u32 v35, v5, 3, v4\n" \
  "v_lshrrev_b32 v4, 3, v1\n" \
  "v_lshlrev_b32 v4, 1, v4\n" \
  "v_lshrrev_b32 v5, 1, v0\n" \
  "v_add_u32 v4, v4, v5\n" \
  "s_lshl_b32 s88, s78, 4\n" \
  "s_lshl_b32 s89, s77, 2\n" \
  "s_add_u32 s88, s88, s89\n" \
  "v_add_u32 v4, s88, v4\n" \
  "v_lshlrev_b32 v36, 14, v4\n" \
  "v_bfe_u32 v5, v1, 2, 1\n" \
  "v_lshl_add_u32 v36, v5, 12, v36\n" \
  "v_and_b32 v5, 3, v1\n" \
  "v_lshl_add_u32 v36, v5, 5, v36\n" \
  "v_and_b32 v5, 1, v0\n" \
  "v_lshl_add_u32 v36, v5, 4, v36\n" \
  "s_lshl_b32 s88, s80, 7\n" \
  "v_add_u32 v36, s88, v36\n" \
  "v_and_b32 v4, 7, v95\n" \
  "v_lshlrev_b32 v4, 3, v4\n" \
  "v_lshrrev_b32 v5, 5, v95\n" \
  "v_lshl_add_u32 v37, v5, 2, v4\n" \
  "v_mov_b32 v39, 0x4444\n" \
  "v_mov_b32 v4, 0xeeee\n" \
  "v_cmp_lt_u32 vcc, 7, v1\n" \
  "s_nop 1\n" \
  "v_cndmask_b32 v39, v39, v4, vcc\n" \
  "v_mov_b32 v80, 0\n" \
  "v_mov_b32 v81, 0\n" \
  "v_mov_b32 v84, 0\n" \
  "v_mov_b32 v85, 0\n" \
  "v_mov_b32 v88, 0\n" \
  "v_mov_b32 v89, 0\n" \
  "v_mov_b32 v90, 0x3f803f80\n" \
  "v_mov_b32 v91, 0x3f803f80\n" \
  "v_mov_b32 v92, 0x3f803f80\n" \
  "v_mov_b32 v93, 0x3f803f80\n" \
  "s_mov_b32 s56, 0x3e0293ee\n" \
  "s_mov_b32 s57, 0x3e0293ee\n" \
  "s_mov_b32 s58, 0xbe0293ee\n" \
  "s_mov_b32 s84, -1\n" \
  "s_mov_b32 s85, 0\n" \
  "s_mov_b32 s86, 0xff00ff00\n" \
  "s_mov_b32 s87, 0xff00ff00\n" \
  "s_mov_b32 s65, 0\n" \
  "s_mov_b32 s50, s81\n" \
  "s_add_u32 s51, s81, 0x100000\n" \
  "s_add_u32 s64, s82, 0x300000\n" \
  "s_lshr_b32 s88, s79, 1\n" \
  "s_lshl_b32 s88, s88, 14\n" \
  "s_and_b32 s89, s79, 1\n" \
  "s_lshl_b32 s89, s89, 12\n" \
  "s_add_u32 s71, s88, s89\n" \
  "s_lshl_b32 s74, s77, 11\n" \
  "s_lshl_b32 s75, s79, 6\n" \
  "s_lshl_b32 s72, s80, 14\n" \
  "s_lshl_b32 s88, s77, 12\n" \
  "s_add_u32 s72, s72, s88\n" \
  "s_lshl_b32 s88, s80, 1\n" \
  "s_add_u32 s88, s88, s78\n" \
  "s_lshl_b32 s88, s88, 7\n" \
  "s_add_u32 s76, s88, 32512\n" \
  "s_and_b32 s89, s78, 1\n" \
  "s_lshl_b32 s88, s80, 1\n" \
  "s_add_u32 s88, s88, s89\n" \
  "s_lshl_b32 s88, s88, 6\n" \
  "s_add_u32 s88, s88, 57088\n" \
  "s_cmp_lt_u32 s78, 2\n" \
  "s_cselect_b32 s76, s76, s88\n" \
  "s_mov_b32 s61, 0\n" \
  "s_mov_b32 s62, 0\n" \
  "s_mov_b32 s91, 1\n" \
  "s_cmp_ge_u32 s61, s91\n" \
  "s_cbranch_scc1 L_KVDI0_%=\n" \
  "s_lshl_b32 s88, s80, 9\n" \
  "s_add_u32 s88, s88, 3840\n" \
  "v_and_b32 v77, 16, v32\n" \
  "v_lshlrev_b32 v77, 4, v77\n" \
  "v_add_u32 v77, s88, v77\n" \
  "L_KVCI0_%=:\n" \
  "global_load_dword v94, v77, s[92:93] sc1\n" \
  "s_waitcnt vmcnt(0)\n" \
  "v_readlane_b32 s61, v94, 0\n" \
  "v_readlane_b32 s89, v94, 1\n" \
  "s_nop 1\n" \
  "s_min_u32 s61, s61, s89\n" \
  "s_cmp_ge_u32 s61, s91\n" \
  "s_cbranch_scc1 L_KVDI0_%=\n" \
  "s_add_u32 s83, s83, 1\n" \
  "s_cmp_gt_u32 s83, 0x100000\n" \
  "s_cbranch_scc1 L_DEAD_%=\n" \
  "s_sleep 2\n" \
  "s_branch L_KVCI0_%=\n" \
  "L_KVDI0_%=:\n" \
  "s_mov_b32 s91, 1\n" \
  "s_cmp_ge_u32 s62, s91\n" \
  "s_cbranch_scc1 L_KVDI1_%=\n" \
  "s_lshl_b32 s88, s80, 9\n" \
  "s_add_u32 s88, s88, 3968\n" \
  "v_and_b32 v77, 16, v32\n" \
  "v_lshlrev_b32 v77, 4, v77\n" \
  "v_add_u32 v77, s88, v77\n" \
  "L_KVCI1_%=:\n" \
  "global_load_dword v94, v77, s[92:93] sc1\n" \
  "s_waitcnt vmcnt(0)\n" \
  "v_readlane_b32 s62, v94, 0\n" \
  "v_readlane_b32 s89, v94, 1\n" \
  "s_nop 1\n" \
  "s_min_u32 s62, s62, s89\n" \
  "s_cmp_ge_u32 s62, s91\n" \
  "s_cbranch_scc1 L_KVDI1_%=\n" \
  "s_add_u32 s83, s83, 1\n" \
  "s_cmp_gt_u32 s83, 0x100000\n" \
  "s_cbranch_scc1 L_DEAD_%=\n" \
  "s_sleep 2\n" \
  "s_branch L_KVCI1_%=\n" \
  "L_KVDI1_%=:\n" \
  "s_lshl_b32 s88, s77, 12\n" \
  "s_add_u32 m0, s88, 8192\n" \
  "s_add_u32 s89, s72, 0\n" \
  "buffer_load_dwordx4 v32, s[44:47], s89 offen sc1 lds\n" \
  "s_add_u32 m0, s88, 9216\n" \
  "s_add_u32 s89, s72, 1024\n" \
  "buffer_load_dwordx4 v32, s[44:47], s89 offen sc1 lds\n" \
  "s_add_u32 m0, s88, 10240\n" \
  "s_add_u32 s89, s72, 2048\n" \
  "buffer_load_dwordx4 v32, s[44:47], s89 offen sc1 lds\n" \
  "s_add_u32 m0, s88, 11264\n" \
  "s_add_u32 s89, s72, 3072\n" \
  "buffer_load_dwordx4 v32, s[44:47], s89 offen sc1 lds\n" \
  "s_lshl_b32 s72, s80, 14\n" \
  "s_waitcnt vmcnt(0)\n" \
  "s_barrier\n" \
  "s_mov_b32 s66, s82\n" \
  "s_mov_b32 s63, 0\n" \
  "v_mov_b32 v40, 0\n" \
  "v_mov_b32 v41, 0\n" \
  "v_mov_b32 v42, 0\n" \
  "v_mov_b32 v43, 0\n" \
  "v_mov_b32 v44, 0\n" \
  "v_mov_b32 v45, 0\n" \
  "v_mov_b32 v46, 0\n" \
  "v_mov_b32 v47, 0\n" \
  "v_mov_b32 v48, 0\n" \
  "v_mov_b32 v49, 0\n" \
  "v_mov_b32 v50, 0\n" \
  "v_mov_b32 v51, 0\n" \
  "v_mov_b32 v52, 0\n" \
  "v_mov_b32 v53, 0\n" \
  "v_mov_b32 v54, 0\n" \
  "v_mov_b32 v55, 0\n" \
  "v_mov_b32 v56, 0\n" \
  "v_mov_b32 v57, 0\n" \
  "v_mov_b32 v58, 0\n" \
  "v_mov_b32 v59, 0\n" \
  "v_mov_b32 v60, 0\n" \
  "v_mov_b32 v61, 0\n" \
  "v_mov_b32 v62, 0\n" \
  "v_mov_b32 v63, 0\n" \
  "s_branch L_ENTRY0_%=\n" \
  "L_TOP0_%=:\n" \
  "s_barrier\n" \
  "s_mov_b32 s91, s48\n" \
  "L_DL1_%=:\n" \
  "s_sub_u32 s91, s91, 1\n" \
  "s_cbranch_scc0 L_DL1_%=\n" \
  "L_ENTRY0_%=:\n" \
  "s_add_u32 s67, s66, s74\n" \
  "buffer_load_dwordx4 v[64:67], v32, s[36:39], s67 offen sc1\n" \
  "buffer_load_dwordx4 v[68:71], v32, s[36:39], s67 offen offset:1024 sc1\n" \
  "s_cmp_eq_u32 s77, 1\n" \
  "s_cbranch_scc1 L_QKW10_%=\n" \
  "s_cmp_eq_u32 s77, 2\n" \
  "s_cbranch_scc1 L_QKW20_%=\n" \
  "s_cmp_eq_u32 s77, 3\n" \
  "s_cbranch_scc1 L_QKW30_%=\n" \
  "L_QKW00_%=:\n" \
  "s_mov_b32 s59, 0\n" \
  "v_add_u32 v94, 0, v33\n" \
  "s_waitcnt vmcnt(0)\n" \
  "L_MOK00_%=:\n" \
  "ds_write_b128 v94, v[64:67] offset:40960\n" \
  "ds_write_b128 v94, v[68:71] offset:41984\n" \
  "v_max3_u32 v76, v64, v65, v66\n" \
  "v_max3_u32 v77, v67, v68, v69\n" \
  "v_max3_u32 v76, v70, v71, v76\n" \
  "v_max_u32 v76, v77, v76\n" \
  "v_cmp_eq_u32 vcc, -1, v76\n" \
  "s_cbranch_vccnz L_C100_%=\n" \
  "v_smfmac_f32_16x16x64_bf16 v[48:51], v[64:67], a[128:135], v39\n" \
  "v_smfmac_f32_16x16x64_bf16 v[52:55], v[64:67], a[192:199], v39\n" \
  "v_smfmac_f32_16x16x64_bf16 v[40:43], v[64:67], a[0:7], v39\n" \
  "v_smfmac_f32_16x16x64_bf16 v[44:47], v[64:67], a[64:71], v39\n" \
  "v_smfmac_f32_16x16x64_bf16 v[48:51], v[68:71], a[136:143], v39\n" \
  "v_smfmac_f32_16x16x64_bf16 v[52:55], v[68:71], a[200:207], v39\n" \
  "v_smfmac_f32_16x16x64_bf16 v[40:43], v[68:71], a[8:15], v39\n" \
  "v_smfmac_f32_16x16x64_bf16 v[44:47], v[68:71], a[72:79], v39\n" \
  "s_waitcnt lgkmcnt(0)\n" \
  "s_barrier\n" \
  "ds_read_b128 v[8:11], v32 offset:43008\n" \
  "ds_read_b128 v[12:15], v32 offset:44032\n" \
  "ds_read_b128 v[16:19], v32 offset:45056\n" \
  "ds_read_b128 v[20:23], v32 offset:46080\n" \
  "ds_read_b128 v[24:27], v32 offset:47104\n" \
  "ds_read_b128 v[28:31], v32 offset:48128\n" \
  "ds_read_b128 v[0:3], v32 offset:40960\n" \
  "ds_read_b128 v[4:7], v32 offset:41984\n" \
  "s_waitcnt lgkmcnt(7)\n" \
  "v_smfmac_f32_16x16x64_bf16 v[48:51], v[8:11], a[144:151], v39\n" \
  "v_smfmac_f32_16x16x64_bf16 v[52:55], v[8:11], a[208:215], v39\n" \
  "v_smfmac_f32_16x16x64_bf16 v[40:43], v[8:11], a[16:23], v39\n" \
  "v_smfmac_f32_16x16x64_bf16 v[44:47], v[8:11], a[80:87], v39\n" \
  "s_waitcnt lgkmcnt(6)\n" \
  "v_smfmac_f32_16x16x64_bf16 v[48:51], v[12:15], a[152:159], v39\n" \
  "v_smfmac_f32_16x16x64_bf16 v[52:55], v[12:15], a[216:223], v39\n" \
  "v_smfmac_f32_16x16x64_bf16 v[40:43], v[12:15], a[24:31], v39\n" \
  "v_smfmac_f32_16x16x64_bf16 v[44:47], v[12:15], a[88:95], v39\n" \
  "s_waitcnt lgkmcnt(5)\n" \
  "v_smfmac_f32_16x16x64_bf16 v[48:51], v[16:19], a[160:167], v39\n" \
  "v_smfmac_f32_16x16x64_bf16 v[52:55], v[16:19], a[224:231], v39\n" \
  "v_smfmac_f32_16x16x64_bf16 v[40:43], v[16:19], a[32:39], v39\n" \
  "v_smfmac_f32_16x16x64_bf16 v[44:47], v[16:19], a[96:103], v39\n" \
  "s_waitcnt lgkmcnt(4)\n" \
  "v_smfmac_f32_16x16x64_bf16 v[48:51], v[20:23], a[168:175], v39\n" \
  "v_smfmac_f32_16x16x64_bf16 v[52:55], v[20:23], a[232:239], v39\n" \
  "v_smfmac_f32_16x16x64_bf16 v[40:43], v[20:23], a[40:47], v39\n" \
  "v_smfmac_f32_16x16x64_bf16 v[44:47], v[20:23], a[104:111], v39\n" \
  "s_waitcnt lgkmcnt(3)\n" \
  "v_smfmac_f32_16x16x64_bf16 v[48:51], v[24:27], a[176:183], v39\n" \
  "v_smfmac_f32_16x16x64_bf16 v[52:55], v[24:27], a[240:247], v39\n" \
  "v_smfmac_f32_16x16x64_bf16 v[40:43], v[24:27], a[48:55], v39\n" \
  "v_smfmac_f32_16x16x64_bf16 v[44:47], v[24:27], a[112:119], v39\n" \
  "s_waitcnt lgkmcnt(2)\n" \
  "v_smfmac_f32_16x16x64_bf16 v[48:51], v[28:31], a[184:191], v39\n" \
  "v_smfmac_f32_16x16x64_bf16 v[52:55], v[28:31], a[248:255], v39\n" \
  "v_smfmac_f32_16x16x64_bf16 v[40:43], v[28:31], a[56:63], v39\n" \
  "v_smfmac_f32_16x16x64_bf16 v[44:47], v[28:31], a[120:127], v39\n" \
  "s_branch L_QKD0_%=\n" \
  ".p2align 6\n" \
  "L_QKW10_%=:\n" \
  "s_mov_b32 s59, 0\n" \
  "v_mov_b32 v77, s76\n" \
  "v_mov_b32 v94, s65\n" \
  "s_mov_b64 exec, 1\n" \
  "global_store_dword v77, v94, s[92:93] sc1\n" \
  "s_mov_b64 exec, -1\n" \
  "v_add_u32 v94, 1024, v33\n" \
  "s_waitcnt vmcnt(1)\n" \
  "L_MOK10_%=:\n" \
  "ds_write_b128 v94, v[64:67] offset:40960\n" \
  "ds_write_b128 v94, v[68:71] offset:41984\n" \
  "v_max3_u32 v76, v64, v65, v66\n" \
  "v_max3_u32 v77, v67, v68, v69\n" \
  "v_max3_u32 v76, v70, v71, v76\n" \
  "v_max_u32 v76, v77, v76\n" \
  "v_cmp_eq_u32 vcc, -1, v76\n" \
  "s_cbranch_vccnz L_C110_%=\n" \
  "v_smfmac_f32_16x16x64_bf16 v[48:51], v[64:67], a[144:151], v39\n" \
  "v_smfmac_f32_16x16x64_bf16 v[52:55], v[64:67], a[208:215], v39\n" \
  "v_smfmac_f32_16x16x64_bf16 v[40:43], v[64:67], a[16:23], v39\n" \
  "v_smfmac_f32_16x16x64_bf16 v[44:47], v[64:67], a[80:87], v39\n" \
  "v_smfmac_f32_16x16x64_bf16 v[48:51], v[68:71], a[152:159], v39\n" \
  "v_smfmac_f32_16x16x64_bf16 v[52:55], v[68:71], a[216:223], v39\n" \
  "v_smfmac_f32_16x16x64_bf16 v[40:43], v[68:71], a[24:31], v39\n" \
  "v_smfmac_f32_16x16x64_bf16 v[44:47], v[68:71], a[88:95], v39\n" \
  "s_waitcnt lgkmcnt(0)\n" \
  "s_barrier\n" \
  "ds_read_b128 v[0:3], v32 offset:40960\n" \
  "ds_read_b128 v[4:7], v32 offset:41984\n" \
  "ds_read_b128 v[16:19], v32 offset:45056\n" \
  "ds_read_b128 v[20:23], v32 offset:46080\n" \
  "ds_read_b128 v[24:27], v32 offset:47104\n" \
  "ds_read_b128 v[28:31], v32 offset:48128\n" \
  "ds_read_b128 v[8:11], v32 offset:43008\n" \
  "ds_read_b128 v[12:15], v32 offset:44032\n" \
  "s_waitcnt lgkmcnt(7)\n" \
  "v_smfmac_f32_16x16x64_bf16 v[48:51], v[0:3], a[128:135], v39\n" \
  "v_smfmac_f32_16x16x64_bf16 v[52:55], v[0:3], a[192:199], v39\n" \
  "s_cmp_lt_u32 s65, 63\n" \
  "v_smfmac_f32_16x16x64_bf16 v[40:43], v[0:3], a[0:7], v39\n" \
  "s_cselect_b32 s90, 0x80000, 0\n" \
  "v_smfmac_f32_16x16x64_bf16 v[44:47], v[0:3], a[64:71], v39\n" \
  "s_add_u32 s72, s72, s90\n" \
  "s_waitcnt lgkmcnt(6)\n" \
  "v_smfmac_f32_16x16x64_bf16 v[48:51], v[4:7], a[136:143], v39\n" \
  "s_mov_b32 m0, 24576\n" \
  "v_smfmac_f32_16x16x64_bf16 v[52:55], v[4:7], a[200:207], v39\n" \
  "s_add_u32 s89, s72, 0\n" \
  "v_smfmac_f32_16x16x64_bf16 v[40:43], v[4:7], a[8:15], v39\n" \
  "buffer_load_dwordx4 v32, s[44:47], s89 offen sc1 lds\n" \
  "v_smfmac_f32_16x16x64_bf16 v[44:47], v[4:7], a[72:79], v39\n" \
  "s_mov_b32 m0, 27648\n" \
  "s_waitcnt lgkmcnt(5)\n" \
  "v_smfmac_f32_16x16x64_bf16 v[48:51], v[16:19], a[160:167], v39\n" \
  "s_add_u32 s89, s72, 3072\n" \
  "v_smfmac_f32_16x16x64_bf16 v[52:55], v[16:19], a[224:231], v39\n" \
  "buffer_load_dwordx4 v32, s[44:47], s89 offen sc1 lds\n" \
  "v_smfmac_f32_16x16x64_bf16 v[40:43], v[16:19], a[32:39], v39\n" \
  "s_mov_b32 m0, 30720\n" \
  "v_smfmac_f32_16x16x64_bf16 v[44:47], v[16:19], a[96:103], v39\n" \
  "s_add_u32 s89, s72, 6144\n" \
  "s_waitcnt lgkmcnt(4)\n" \
  "v_smfmac_f32_16x16x64_bf16 v[48:51], v[20:23], a[168:175], v39\n" \
  "buffer_load_dwordx4 v32, s[44:47], s89 offen sc1 lds\n" \
  "v_smfmac_f32_16x16x64_bf16 v[52:55], v[20:23], a[232:239], v39\n" \
  "s_mov_b32 m0, 33792\n" \
  "v_smfmac_f32_16x16x64_bf16 v[40:43], v[20:23], a[40:47], v39\n" \
  "s_add_u32 s89, s72, 9216\n" \
  "v_smfmac_f32_16x16x64_bf16 v[44:47], v[20:23], a[104:111], v39\n" \
  "buffer_load_dwordx4 v32, s[44:47], s89 offen sc1 lds\n" \
  "s_waitcnt lgkmcnt(3)\n" \
  "v_smfmac_f32_16x16x64_bf16 v[48:51], v[24:27], a[176:183], v39\n" \
  "s_mov_b32 m0, 36864\n" \
  "v_smfmac_f32_16x16x64_bf16 v[52:55], v[24:27], a[240:247], v39\n" \
  "s_add_u32 s89, s72, 12288\n" \
  "v_smfmac_f32_16x16x64_bf16 v[40:43], v[24:27], a[48:55], v39\n" \
  "buffer_load_dwordx4 v32, s[44:47], s89 offen sc1 lds\n" \
  "v_smfmac_f32_16x16x64_bf16 v[44:47], v[24:27], a[112:119], v39\n" \
  "s_mov_b32 m0, 39936\n" \
  "s_waitcnt lgkmcnt(2)\n" \
  "v_smfmac_f32_16x16x64_bf16 v[48:51], v[28:31], a[184:191], v39\n" \
  "s_add_u32 s89, s72, 15360\n" \
  "v_smfmac_f32_16x16x64_bf16 v[52:55], v[28:31], a[248:255], v39\n" \
  "buffer_load_dwordx4 v32, s[44:47], s89 offen sc1 lds\n" \
  "v_smfmac_f32_16x16x64_bf16 v[40:43], v[28:31], a[56:63], v39\n" \
  "v_smfmac_f32_16x16x64_bf16 v[44:47], v[28:31], a[120:127], v39\n" \
  "s_branch L_QKD0_%=\n" \
  ".p2align 6\n" \
  "L_QKW20_%=:\n" \
  "s_mov_b32 s59, 0\n" \
  "v_add_u32 v94, 2048, v33\n" \
  "s_waitcnt vmcnt(0)\n" \
  "L_MOK20_%=:\n" \
  "ds_write_b128 v94, v[64:67] offset:40960\n" \
  "ds_write_b128 v94, v[68:71] offset:41984\n" \
  "v_max3_u32 v76, v64, v65, v66\n" \
  "v_max3_u32 v77, v67, v68, v69\n" \
  "v_max3_u32 v76, v70, v71, v76\n" \
  "v_max_u32 v76, v77, v76\n" \
  "v_cmp_eq_u32 vcc, -1, v76\n" \
  "s_cbranch_vccnz L_C120_%=\n" \
  "v_smfmac_f32_16x16x64_bf16 v[48:51], v[64:67], a[160:167], v39\n" \
  "v_smfmac_f32_16x16x64_bf16 v[52:55], v[64:67], a[224:231], v39\n" \
  "v_smfmac_f32_16x16x64_bf16 v[40:43], v[64:67], a[32:39], v39\n" \
  "v_smfmac_f32_16x16x64_bf16 v[44:47], v[64:67], a[96:103], v39\n" \
  "v_smfmac_f32_16x16x64_bf16 v[48:51], v[68:71], a[168:175], v39\n" \
  "v_smfmac_f32_16x16x64_bf16 v[52:55], v[68:71], a[232:239], v39\n" \
  "v_smfmac_f32_16x16x64_bf16 v[40:43], v[68:71], a[40:47], v39\n" \
  "v_smfmac_f32_16x16x64_bf16 v[44:47], v[68:71], a[104:111], v39\n" \
  "s_waitcnt lgkmcnt(0)\n" \
  "s_barrier\n" \
  "ds_read_b128 v[0:3], v32 offset:40960\n" \
  "ds_read_b128 v[4:7], v32 offset:41984\n" \
  "ds_read_b128 v[8:11], v32 offset:43008\n" \
  "ds_read_b128 v[12:15], v32 offset:44032\n" \
  "ds_read_b128 v[24:27], v32 offset:47104\n" \
  "ds_read_b128 v[28:31], v32 offset:48128\n" \
  "ds_read_b128 v[16:19], v32 offset:45056\n" \
  "ds_read_b128 v[20:23], v32 offset:46080\n" \
  "s_waitcnt lgkmcnt(7)\n" \
  "v_smfmac_f32_16x16x64_bf16 v[48:51], v[0:3], a[128:135], v39\n" \
  "v_smfmac_f32_16x16x64_bf16 v[52:55], v[0:3], a[192:199], v39\n" \
  "s_cmp_lt_u32 s65, 63\n" \
  "v_smfmac_f32_16x16x64_bf16 v[40:43], v[0:3], a[0:7], v39\n" \
  "s_cselect_b32 s90, 0x80000, 0\n" \
  "v_smfmac_f32_16x16x64_bf16 v[44:47], v[0:3], a[64:71], v39\n" \
  "s_add_u32 s72, s72, s90\n" \
  "s_waitcnt lgkmcnt(6)\n" \
  "v_smfmac_f32_16x16x64_bf16 v[48:51], v[4:7], a[136:143], v39\n" \
  "s_mov_b32 m0, 25600\n" \
  "v_smfmac_f32_16x16x64_bf16 v[52:55], v[4:7], a[200:207], v39\n" \
  "s_add_u32 s89, s72, 1024\n" \
  "v_smfmac_f32_16x16x64_bf16 v[40:43], v[4:7], a[8:15], v39\n" \
  "buffer_load_dwordx4 v32, s[44:47], s89 offen sc1 lds\n" \
  "v_smfmac_f32_16x16x64_bf16 v[44:47], v[4:7], a[72:79], v39\n" \
  "s_mov_b32 m0, 28672\n" \
  "s_waitcnt lgkmcnt(5)\n" \
  "v_smfmac_f32_16x16x64_bf16 v[48:51], v[8:11], a[144:151], v39\n" \
  "s_add_u32 s89, s72, 4096\n" \
  "v_smfmac_f32_16x16x64_bf16 v[52:55], v[8:11], a[208:215], v39\n" \
  "buffer_load_dwordx4 v32, s[44:47], s89 offen sc1 lds\n" \
  "v_smfmac_f32_16x16x64_bf16 v[40:43], v[8:11], a[16:23], v39\n" \
  "s_mov_b32 m0, 31744\n" \
  "v_smfmac_f32_16x16x64_bf16 v[44:47], v[8:11], a[80:87], v39\n" \
  "s_add_u32 s89, s72, 7168\n" \
  "s_waitcnt lgkmcnt(4)\n" \
  "v_smfmac_f32_16x16x64_bf16 v[48:51], v[12:15], a[152:159], v39\n" \
  "buffer_load_dwordx4 v32, s[44:47], s89 offen sc1 lds\n" \
  "v_smfmac_f32_16x16x64_bf16 v[52:55], v[12:15], a[216:223], v39\n" \
  "s_mov_b32 m0, 34816\n" \
  "v_smfmac_f32_16x16x64_bf16 v[40:43], v[12:15], a[24:31], v39\n" \
  "s_add_u32 s89, s72, 10240\n" \
  "v_smfmac_f32_16x16x64_bf16 v[44:47], v[12:15], a[88:95], v39\n" \
  "buffer_load_dwordx4 v32, s[44:47], s89 offen sc1 lds\n" \
  "s_waitcnt lgkmcnt(3)\n" \
  "v_smfmac_f32_16x16x64_bf16 v[48:51], v[24:27], a[176:183], v39\n" \
  "s_mov_b32 m0, 37888\n" \
  "v_smfmac_f32_16x16x64_bf16 v[52:55], v[24:27], a[240:247], v39\n" \
  "s_add_u32 s89, s72, 13312\n" \
  "v_smfmac_f32_16x16x64_bf16 v[40:43], v[24:27], a[48:55], v39\n" \
  "buffer_load_dwordx4 v32, s[44:47], s89 offen sc1 lds\n" \
  "v_smfmac_f32_16x16x64_bf16 v[44:47], v[24:27], a[112:119], v39\n" \
  "s_waitcnt lgkmcnt(2)\n" \
  "v_smfmac_f32_16x16x64_bf16 v[48:51], v[28:31], a[184:191], v39\n" \
  "v_smfmac_f32_16x16x64_bf16 v[52:55], v[28:31], a[248:255], v39\n" \
  "v_smfmac_f32_16x16x64_bf16 v[40:43], v[28:31], a[56:63], v39\n" \
  "v_smfmac_f32_16x16x64_bf16 v[44:47], v[28:31], a[120:127], v39\n" \
  "s_branch L_QKD0_%=\n" \
  ".p2align 6\n" \
  "L_QKW30_%=:\n" \
  "s_mov_b32 s59, 0\n" \
  "v_add_u32 v94, 3072, v33\n" \
  "s_waitcnt vmcnt(0)\n" \
  "L_MOK30_%=:\n" \
  "ds_write_b128 v94, v[64:67] offset:40960\n" \
  "ds_write_b128 v94, v[68:71] offset:41984\n" \
  "v_max3_u32 v76, v64, v65, v66\n" \
  "v_max3_u32 v77, v67, v68, v69\n" \
  "v_max3_u32 v76, v70, v71, v76\n" \
  "v_max_u32 v76, v77, v76\n" \
  "v_cmp_eq_u32 vcc, -1, v76\n" \
  "s_cbranch_vccnz L_C130_%=\n" \
  "v_smfmac_f32_16x16x64_bf16 v[48:51], v[64:67], a[176:183], v39\n" \
  "v_smfmac_f32_16x16x64_bf16 v[52:55], v[64:67], a[240:247], v39\n" \
  "v_smfmac_f32_16x16x64_bf16 v[40:43], v[64:67], a[48:55], v39\n" \
  "v_smfmac_f32_16x16x64_bf16 v[44:47], v[64:67], a[112:119], v39\n" \
  "v_smfmac_f32_16x16x64_bf16 v[48:51], v[68:71], a[184:191], v39\n" \
  "v_smfmac_f32_16x16x64_bf16 v[52:55], v[68:71], a[248:255], v39\n" \
  "v_smfmac_f32_16x16x64_bf16 v[40:43], v[68:71], a[56:63], v39\n" \
  "v_smfmac_f32_16x16x64_bf16 v[44:47], v[68:71], a[120:127], v39\n" \
  "s_waitcnt lgkmcnt(0)\n" \
  "s_barrier\n" \
  "ds_read_b128 v[0:3], v32 offset:40960\n" \
  "ds_read_b128 v[4:7], v32 offset:41984\n" \
  "ds_read_b128 v[8:11], v32 offset:43008\n" \
  "ds_read_b128 v[12:15], v32 offset:44032\n" \
  "ds_read_b128 v[16:19], v32 offset:45056\n" \
  "ds_read_b128 v[20:23], v32 offset:46080\n" \
  "ds_read_b128 v[24:27], v32 offset:47104\n" \
  "ds_read_b128 v[28:31], v32 offset:48128\n" \
  "s_waitcnt lgkmcnt(7)\n" \
  "v_smfmac_f32_16x16x64_bf16 v[48:51], v[0:3], a[128:135], v39\n" \
  "v_smfmac_f32_16x16x64_bf16 v[52:55], v[0:3], a[192:199], v39\n" \
  "s_cmp_lt_u32 s65, 63\n" \
  "v_smfmac_f32_16x16x64_bf16 v[40:43], v[0:3], a[0:7], v39\n" \
  "s_cselect_b32 s90, 0x80000, 0\n" \
  "v_smfmac_f32_16x16x64_bf16 v[44:47], v[0:3], a[64:71], v39\n" \
  "s_add_u32 s72, s72, s90\n" \
  "s_waitcnt lgkmcnt(6)\n" \
  "v_smfmac_f32_16x16x64_bf16 v[48:51], v[4:7], a[136:143], v39\n" \
  "s_mov_b32 m0, 26624\n" \
  "v_smfmac_f32_16x16x64_bf16 v[52:55], v[4:7], a[200:207], v39\n" \
  "s_add_u32 s89, s72, 2048\n" \
  "v_smfmac_f32_16x16x64_bf16 v[40:43], v[4:7], a[8:15], v39\n" \
  "buffer_load_dwordx4 v32, s[44:47], s89 offen sc1 lds\n" \
  "v_smfmac_f32_16x16x64_bf16 v[44:47], v[4:7], a[72:79], v39\n" \
  "s_mov_b32 m0, 29696\n" \
  "s_waitcnt lgkmcnt(5)\n" \
  "v_smfmac_f32_16x16x64_bf16 v[48:51], v[8:11], a[144:151], v39\n" \
  "s_add_u32 s89, s72, 5120\n" \
  "v_smfmac_f32_16x16x64_bf16 v[52:55], v[8:11], a[208:215], v39\n" \
  "buffer_load_dwordx4 v32, s[44:47], s89 offen sc1 lds\n" \
  "v_smfmac_f32_16x16x64_bf16 v[40:43], v[8:11], a[16:23], v39\n" \
  "s_mov_b32 m0, 32768\n" \
  "v_smfmac_f32_16x16x64_bf16 v[44:47], v[8:11], a[80:87], v39\n" \
  "s_add_u32 s89, s72, 8192\n" \
  "s_waitcnt lgkmcnt(4)\n" \
  "v_smfmac_f32_16x16x64_bf16 v[48:51], v[12:15], a[152:159], v39\n" \
  "buffer_load_dwordx4 v32, s[44:47], s89 offen sc1 lds\n" \
  "v_smfmac_f32_16x16x64_bf16 v[52:55], v[12:15], a[216:223], v39\n" \
  "s_mov_b32 m0, 35840\n" \
  "v_smfmac_f32_16x16x64_bf16 v[40:43], v[12:15], a[24:31], v39\n" \
  "s_add_u32 s89, s72, 11264\n" \
  "v_smfmac_f32_16x16x64_bf16 v[44:47], v[12:15], a[88:95], v39\n" \
  "buffer_load_dwordx4 v32, s[44:47], s89 offen sc1 lds\n" \
  "s_waitcnt lgkmcnt(3)\n" \
  "v_smfmac_f32_16x16x64_bf16 v[48:51], v[16:19], a[160:167], v39\n" \
  "s_mov_b32 m0, 38912\n" \
  "v_smfmac_f32_16x16x64_bf16 v[52:55], v[16:19], a[224:231], v39\n" \
  "s_add_u32 s89, s72, 14336\n" \
  "v_smfmac_f32_16x16x64_bf16 v[40:43], v[16:19], a[32:39], v39\n" \
  "buffer_load_dwordx4 v32, s[44:47], s89 offen sc1 lds\n" \
  "v_smfmac_f32_16x16x64_bf16 v[44:47], v[16:19], a[96:103], v39\n" \
  "s_waitcnt lgkmcnt(2)\n" \
  "v_smfmac_f32_16x16x64_bf16 v[48:51], v[20:23], a[168:175], v39\n" \
  "v_smfmac_f32_16x16x64_bf16 v[52:55], v[20:23], a[232:239], v39\n" \
  "v_smfmac_f32_16x16x64_bf16 v[40:43], v[20:23], a[40:47], v39\n" \
  "v_smfmac_f32_16x16x64_bf16 v[44:47], v[20:23], a[104:111], v39\n" \
  "s_branch L_QKD0_%=\n" \
  ".p2align 6\n" \
  "L_QKD0_%=:\n" \
  "s_waitcnt lgkmcnt(0)\n" \
  "v_smfmac_f32_16x16x64_bf16 v[56:59], v[0:3], v[128:135], v39\n" \
  "v_smfmac_f32_16x16x64_bf16 v[60:63], v[0:3], v[192:199], v39\n" \
  "s_nop 4\n" \
  "v_permlane32_swap_b32 v48, v52\n" \
  "v_permlane32_swap_b32 v49, v53\n" \
  "v_permlane32_swap_b32 v50, v54\n" \
  "v_permlane32_swap_b32 v51, v55\n" \
  "v_smfmac_f32_16x16x64_bf16 v[56:59], v[4:7], v[136:143], v39\n" \
  "v_pk_add_f32 v[48:49], v[48:49], v[52:53]\n" \
  "v_pk_add_f32 v[50:51], v[50:51], v[54:55]\n" \
  "v_permlane32_swap_b32 v40, v44\n" \
  "v_permlane32_swap_b32 v41, v45\n" \
  "v_cvt_pk_bf16_f32 v52, v48, v49\n" \
  "v_cvt_pk_bf16_f32 v53, v50, v51\n" \
  "v_smfmac_f32_16x16x64_bf16 v[60:63], v[4:7], v[200:207], v39\n" \
  "v_permlane32_swap_b32 v42, v46\n" \
  "v_permlane32_swap_b32 v43, v47\n" \
  "ds_write_b64 v34, v[52:53] offset:2048\n" \
  "v_pk_add_f32 v[40:41], v[40:41], v[44:45]\n" \
  "v_pk_add_f32 v[42:43], v[42:43], v[46:47]\n" \
  "v_cvt_pk_bf16_f32 v44, v40, v41\n" \
  "v_cvt_pk_bf16_f32 v45, v42, v43\n" \
  "ds_write_b64 v34, v[44:45] offset:0\n" \
  "v_smfmac_f32_16x16x64_bf16 v[56:59], v[8:11], v[144:151], v39\n" \
  "v_smfmac_f32_16x16x64_bf16 v[60:63], v[8:11], v[208:215], v39\n" \
  "v_smfmac_f32_16x16x64_bf16 v[56:59], v[12:15], v[152:159], v39\n" \
  "v_smfmac_f32_16x16x64_bf16 v[60:63], v[12:15], v[216:223], v39\n" \
  "s_waitcnt lgkmcnt(0)\n" \
  "s_barrier\n" \
  "ds_read_b64_tr_b16 v[40:41], v35 offset:0\n" \
  "ds_read_b64_tr_b16 v[42:43], v35 offset:256\n" \
  "v_smfmac_f32_16x16x64_bf16 v[56:59], v[16:19], v[160:167], v39\n" \
  "ds_read_b64_tr_b16 v[44:45], v35 offset:2048\n" \
  "ds_read_b64_tr_b16 v[46:47], v35 offset:2304\n" \
  "v_smfmac_f32_16x16x64_bf16 v[60:63], v[16:19], v[224:231], v39\n" \
  "ds_read_b128 v[0:3], v32 offset:8192\n" \
  "ds_read_b128 v[4:7], v32 offset:12288\n" \
  "v_smfmac_f32_16x16x64_bf16 v[56:59], v[20:23], v[168:175], v39\n" \
  "ds_read_b64_tr_b16 v[48:49], v35 offset:512\n" \
  "ds_read_b64_tr_b16 v[50:51], v35 offset:768\n" \
  "v_smfmac_f32_16x16x64_bf16 v[60:63], v[20:23], v[232:239], v39\n" \
  "ds_read_b64_tr_b16 v[52:53], v35 offset:2560\n" \
  "ds_read_b64_tr_b16 v[54:55], v35 offset:2816\n" \
  "v_smfmac_f32_16x16x64_bf16 v[56:59], v[24:27], v[176:183], v39\n" \
  "ds_read_b128 v[8:11], v32 offset:9216\n" \
  "ds_read_b128 v[12:15], v32 offset:13312\n" \
  "v_smfmac_f32_16x16x64_bf16 v[60:63], v[24:27], v[240:247], v39\n" \
  "v_smfmac_f32_16x16x64_bf16 v[56:59], v[28:31], v[184:191], v39\n" \
  "v_smfmac_f32_16x16x64_bf16 v[60:63], v[28:31], v[248:255], v39\n" \
  "s_waitcnt lgkmcnt(6)\n" \
  "v_mfma_f32_16x16x32_bf16 v[64:67], v[44:47], v[40:43], 0\n" \
  "ds_read_b64_tr_b16 v[44:45], v35 offset:3072\n" \
  "ds_read_b64_tr_b16 v[46:47], v35 offset:3328\n" \
  "v_mfma_f32_16x16x32_bf16 v[68:71], v[0:3], v[40:43], 0\n" \
  "ds_read_b128 v[16:19], v32 offset:10240\n" \
  "ds_read_b128 v[20:23], v32 offset:14336\n" \
  "v_mfma_f32_16x16x32_bf16 v[72:75], v[4:7], v[40:43], 0\n" \
  "ds_read_b64_tr_b16 v[40:41], v35 offset:1024\n" \
  "ds_read_b64_tr_b16 v[42:43], v35 offset:1280\n" \
  "s_waitcnt lgkmcnt(6)\n" \
  "v_mfma_f32_16x16x32_bf16 v[64:67], v[52:55], v[48:51], v[64:67]\n" \
  "ds_read_b64_tr_b16 v[52:53], v35 offset:3584\n" \
  "ds_read_b64_tr_b16 v[54:55], v35 offset:3840\n" \
  "v_mfma_f32_16x16x32_bf16 v[68:71], v[8:11], v[48:51], v[68:71]\n" \
  "ds_read_b128 v[24:27], v32 offset:11264\n" \
  "ds_read_b128 v[28:31], v32 offset:15360\n" \
  "v_mfma_f32_16x16x32_bf16 v[72:75], v[12:15], v[48:51], v[72:75]\n" \
  "ds_read_b64_tr_b16 v[48:49], v35 offset:1536\n" \
  "ds_read_b64_tr_b16 v[50:51], v35 offset:1792\n" \
  "ds_read_b128 v[0:3], v33 offset:16384\n" \
  "ds_read_b128 v[4:7], v33 offset:20480\n" \
  "s_waitcnt lgkmcnt(8)\n" \
  "v_mfma_f32_16x16x32_bf16 v[64:67], v[44:47], v[40:43], v[64:67]\n" \
  "v_permlane32_swap_b32 v56, v60\n" \
  "v_mfma_f32_16x16x32_bf16 v[68:71], v[16:19], v[40:43], v[68:71]\n" \
  "v_permlane32_swap_b32 v57, v61\n" \
  "v_mfma_f32_16x16x32_bf16 v[72:75], v[20:23], v[40:43], v[72:75]\n" \
  "v_permlane32_swap_b32 v58, v62\n" \
  "s_waitcnt lgkmcnt(2)\n" \
  "v_mfma_f32_16x16x32_bf16 v[64:67], v[52:55], v[48:51], v[64:67]\n" \
  "v_permlane32_swap_b32 v59, v63\n" \
  "v_mfma_f32_16x16x32_bf16 v[68:71], v[24:27], v[48:51], v[68:71]\n" \
  "v_pk_add_f32 v[56:57], v[56:57], v[60:61]\n" \
  "v_mfma_f32_16x16x32_bf16 v[72:75], v[28:31], v[48:51], v[72:75]\n" \
  "v_pk_add_f32 v[58:59], v[58:59], v[62:63]\n" \
  "s_cmp_eq_u32 s65, 63\n" \
  "v_cvt_pk_bf16_f32 v78, v56, v57\n" \
  "v_cvt_pk_bf16_f32 v79, v58, v59\n" \
  "s_cbranch_scc1 L_LAST_%=\n" \
  "s_add_u32 s70, s50, s71\n" \
  "s_nop 1\n" \
  "v_max3_f32 v77, v64, v65, v66\n" \
  "v_max3_f32 v94, v67, v68, v69\n" \
  "v_max3_f32 v95, v70, v71, v72\n" \
  "v_max3_f32 v77, v77, v73, v74\n" \
  "v_max3_f32 v94, v94, v75, v95\n" \
  "v_max_f32 v95, v77, v94\n" \
  "v_max_f32 v77, v77, v94\n" \
  "s_nop 1\n" \
  "v_permlane16_swap_b32 v77, v95\n" \
  "v_max_f32 v94, v77, v95\n" \
  "v_max_f32 v77, v77, v95\n" \
  "s_nop 1\n" \
  "v_permlane32_swap_b32 v77, v94\n" \
  "v_max_f32 v77, v77, v94\n" \
  "v_mul_f32 v94, s58, v77\n" \
  "v_pk_fma_f32 v[64:65], v[64:65], s[56:57], v[94:95] op_sel_hi:[1,1,0]\n" \
  "v_pk_fma_f32 v[66:67], v[66:67], s[56:57], v[94:95] op_sel_hi:[1,1,0]\n" \
  "v_pk_fma_f32 v[68:69], v[68:69], s[56:57], v[94:95] op_sel_hi:[1,1,0]\n" \
  "v_pk_fma_f32 v[70:71], v[70:71], s[56:57], v[94:95] op_sel_hi:[1,1,0]\n" \
  "v_pk_fma_f32 v[72:73], v[72:73], s[56:57], v[94:95] op_sel_hi:[1,1,0]\n" \
  "v_pk_fma_f32 v[74:75], v[74:75], s[56:57], v[94:95] op_sel_hi:[1,1,0]\n" \
  "v_exp_f32 v64, v64\n" \
  "v_exp_f32 v65, v65\n" \
  "v_exp_f32 v66, v66\n" \
  "v_exp_f32 v67, v67\n" \
  "v_exp_f32 v68, v68\n" \
  "v_cvt_pk_bf16_f32 v94, v64, v65\n" \
  "v_cvt_pk_bf16_f32 v95, v66, v67\n" \
  "v_exp_f32 v69, v69\n" \
  "v_cndmask_b32 v82, 0, v94, s[84:85]\n" \
  "v_cndmask_b32 v83, 0, v95, s[84:85]\n" \
  "v_cndmask_b32 v86, v94, 0, s[84:85]\n" \
  "v_cndmask_b32 v87, v95, 0, s[84:85]\n" \
  "v_exp_f32 v70, v70\n" \
  "v_mfma_f32_16x16x32_bf16 v[44:47], v[78:81], v[82:85], 0\n" \
  "v_exp_f32 v71, v71\n" \
  "v_exp_f32 v72, v72\n" \
  "v_mfma_f32_16x16x32_bf16 v[48:51], v[78:81], v[86:89], 0\n" \
  "v_exp_f32 v73, v73\n" \
  "v_exp_f32 v74, v74\n" \
  "v_mfma_f32_16x16x32_bf16 v[56:59], v[90:93], v[82:85], 0\n" \
  "v_exp_f32 v75, v75\n" \
  "v_cvt_pk_bf16_f32 v40, v68, v69\n" \
  "v_cvt_pk_bf16_f32 v41, v70, v71\n" \
  "v_cvt_pk_bf16_f32 v42, v72, v73\n" \
  "v_cvt_pk_bf16_f32 v43, v74, v75\n" \
  "s_waitcnt lgkmcnt(0)\n" \
  "s_nop 0\n" \
  "v_mfma_f32_16x16x32_bf16 v[44:47], v[0:3], v[40:43], v[44:47]\n" \
  "v_mfma_f32_16x16x32_bf16 v[48:51], v[4:7], v[40:43], v[48:51]\n" \
  "v_mfma_f32_16x16x32_bf16 v[56:59], v[90:93], v[40:43], v[56:59]\n" \
  "s_nop 5\n" \
  "v_cvt_pk_bf16_f32 v52, v44, v45\n" \
  "v_cvt_pk_bf16_f32 v53, v46, v47\n" \
  "v_cvt_pk_bf16_f32 v54, v48, v49\n" \
  "v_cvt_pk_bf16_f32 v55, v50, v51\n" \
  "ds_write_b128 v33, v[52:55] offset:4096\n" \
  "v_rcp_f32 v77, v56\n" \
  "s_waitcnt lgkmcnt(0)\n" \
  "s_barrier\n" \
  "ds_read_b128 v[8:11], v32 offset:4096\n" \
  "ds_read_b128 v[12:15], v32 offset:5120\n" \
  "ds_read_b128 v[16:19], v32 offset:6144\n" \
  "ds_read_b128 v[20:23], v32 offset:7168\n" \
  "s_waitcnt lgkmcnt(3)\n" \
  "v_mfma_f32_16x16x32_bf16 v[56:59], v[96:99], v[8:11], 0\n" \
  "v_mfma_f32_16x16x32_bf16 v[60:63], v[112:115], v[8:11], 0\n" \
  "s_waitcnt lgkmcnt(2)\n" \
  "v_mfma_f32_16x16x32_bf16 v[56:59], v[100:103], v[12:15], v[56:59]\n" \
  "v_mfma_f32_16x16x32_bf16 v[60:63], v[116:119], v[12:15], v[60:63]\n" \
  "s_waitcnt lgkmcnt(1)\n" \
  "v_mfma_f32_16x16x32_bf16 v[56:59], v[104:107], v[16:19], v[56:59]\n" \
  "v_mfma_f32_16x16x32_bf16 v[60:63], v[120:123], v[16:19], v[60:63]\n" \
  "s_waitcnt lgkmcnt(0)\n" \
  "v_mfma_f32_16x16x32_bf16 v[56:59], v[108:111], v[20:23], v[56:59]\n" \
  "v_mfma_f32_16x16x32_bf16 v[60:63], v[124:127], v[20:23], v[60:63]\n" \
  "s_nop 7\n" \
  "v_cndmask_b32 v64, v56, v60, s[86:87]\n" \
  "v_cndmask_b32 v65, v57, v61, s[86:87]\n" \
  "v_cndmask_b32 v66, v58, v62, s[86:87]\n" \
  "v_cndmask_b32 v67, v59, v63, s[86:87]\n" \
  "v_pk_mul_f32 v[64:65], v[64:65], v[76:77] op_sel:[0,1] op_sel_hi:[1,1]\n" \
  "v_pk_mul_f32 v[66:67], v[66:67], v[76:77] op_sel:[0,1] op_sel_hi:[1,1]\n" \
  "buffer_store_dwordx4 v[64:67], v36, s[40:43], s50 offen sc1\n" \
  "s_cmp_eq_u32 s77, 0\n" \
  "s_cbranch_scc0 L_HELP0_%=\n" \
  "s_lshl_b32 s88, s65, 5\n" \
  "s_add_u32 s88, s88, s80\n" \
  "s_lshl_b32 s73, s88, 11\n" \
  "s_cmp_eq_u32 s78, 0\n" \
  "s_cbranch_scc1 L_PE0_%=a\n" \
  "v_lshrrev_b32 v77, 1, v33\n" \
  "s_nop 0\n" \
  "buffer_store_dwordx2 v[78:79], v77, s[52:55], s73 offen sc1\n" \
  "L_PE0_%=a:\n" \
  "v_mov_b32 v40, 0\n" \
  "v_mov_b32 v41, 0\n" \
  "v_mov_b32 v42, 0\n" \
  "v_mov_b32 v43, 0\n" \
  "v_mov_b32 v44, 0\n" \
  "v_mov_b32 v45, 0\n" \
  "v_mov_b32 v46, 0\n" \
  "v_mov_b32 v47, 0\n" \
  "v_mov_b32 v48, 0\n" \
  "v_mov_b32 v49, 0\n" \
  "v_mov_b32 v50, 0\n" \
  "v_mov_b32 v51, 0\n" \
  "v_mov_b32 v52, 0\n" \
  "v_mov_b32 v53, 0\n" \
  "v_mov_b32 v54, 0\n" \
  "v_mov_b32 v55, 0\n" \
  "v_mov_b32 v56, 0\n" \
  "v_mov_b32 v57, 0\n" \
  "v_mov_b32 v58, 0\n" \
  "v_mov_b32 v59, 0\n" \
  "v_mov_b32 v60, 0\n" \
  "v_mov_b32 v61, 0\n" \
  "v_mov_b32 v62, 0\n" \
  "v_mov_b32 v63, 0\n" \
  "s_mov_b32 s88, s64\n" \
  "s_mov_b32 s64, s50\n" \
  "s_mov_b32 s50, s51\n" \
  "s_mov_b32 s51, s88\n" \
  "s_add_u32 s88, s65, 1\n" \
  "s_and_b32 s88, s88, 3\n" \
  "s_lshl_b32 s88, s88, 16\n" \
  "s_add_u32 s89, s88, s82\n" \
  "s_add_u32 s68, s89, s75\n" \
  "s_add_u32 s69, s66, s75\n" \
  "s_mov_b32 s66, s89\n" \
  "s_mov_b32 s91, s49\n" \
  "L_DL2_%=:\n" \
  "s_sub_u32 s91, s91, 1\n" \
  "s_cbranch_scc0 L_DL2_%=\n" \
  "L_PPOLL0_%=:\n" \
  "buffer_load_dwordx4 v[0:3], v32, s[40:43], s70 offen offset:0 sc1\n" \
  "buffer_load_dwordx4 v[4:7], v32, s[40:43], s70 offen offset:1024 sc1\n" \
  "buffer_load_dwordx4 v[8:11], v32, s[40:43], s70 offen offset:2048 sc1\n" \
  "buffer_load_dwordx4 v[12:15], v32, s[40:43], s70 offen offset:3072 sc1\n" \
  "s_waitcnt vmcnt(2)\n" \
  "v_max3_u32 v77, v0, v1, v2\n" \
  "v_max3_u32 v94, v3, v4, v5\n" \
  "v_pk_add_f32 v[16:17], v[0:1], v[4:5]\n" \
  "v_max3_u32 v77, v6, v7, v77\n" \
  "v_pk_add_f32 v[18:19], v[2:3], v[6:7]\n" \
  "s_waitcnt vmcnt(0)\n" \
  "v_max3_u32 v94, v8, v9, v94\n" \
  "v_max3_u32 v77, v10, v11, v77\n" \
  "v_pk_add_f32 v[20:21], v[8:9], v[12:13]\n" \
  "v_max3_u32 v94, v12, v13, v94\n" \
  "v_max3_u32 v77, v14, v15, v77\n" \
  "v_pk_add_f32 v[22:23], v[10:11], v[14:15]\n" \
  "v_max_u32 v77, v77, v94\n" \
  "v_pk_add_f32 v[16:17], v[16:17], v[20:21]\n" \
  "v_cmp_eq_u32 vcc, -1, v77\n" \
  "v_pk_add_f32 v[18:19], v[18:19], v[22:23]\n" \
  "s_cbranch_vccnz L_RETRYP0_%=\n" \
  "s_nop 0\n" \
  "v_permlane16_swap_b32 v16, v17\n" \
  "v_permlane16_swap_b32 v18, v19\n" \
  "v_add_f32 v16, v16, v17\n" \
  "v_add_f32 v18, v18, v19\n" \
  "s_nop 1\n" \
  "v_permlane32_swap_b32 v16, v18\n" \
  "v_add_f32 v16, v16, v18\n" \
  "s_nop 1\n" \
  "v_add_f32_dpp v16, v16, v16 row_ror:8 row_mask:0xf bank_mask:0xf\n" \
  "v_mov_b32 v17, v16\n" \
  "s_nop 1\n" \
  "v_permlane16_swap_b32 v16, v17\n" \
  "s_nop 0\n" \
  "v_cvt_pk_bf16_f32 v16, v16, v17\n" \
  "v_min_u32 v16, -2, v16\n" \
  "s_mov_b32 exec_lo, 0xff\n" \
  "s_mov_b32 exec_hi, 0xff\n" \
  "buffer_store_dword v16, v37, s[36:39], s68 offen sc1\n" \
  "s_add_u32 s89, s68, 16384\n" \
  "buffer_store_dword v16, v37, s[36:39], s89 offen sc1\n" \
  "s_mov_b64 exec, -1\n" \
  "s_branch L_END0_%=\n" \
  ".p2align 6\n" \
  "L_HELP0_%=:\n" \
  "s_lshl_b32 s88, s65, 5\n" \
  "s_add_u32 s88, s88, s80\n" \
  "s_lshl_b32 s73, s88, 11\n" \
  "s_cmp_eq_u32 s78, 0\n" \
  "s_cbranch_scc0 L_PV0_%=b\n" \
  "s_lshl_b32 s88, s77, 9\n" \
  "v_add_u32 v77, s88, v35\n" \
  "ds_read_b64_tr_b16 v[68:69], v77 offset:2048\n" \
  "ds_read_b64_tr_b16 v[70:71], v77 offset:2304\n" \
  "v_lshrrev_b32 v94, 8, v32\n" \
  "v_and_b32 v95, 0x70, v32\n" \
  "v_lshl_add_u32 v94, v94, 7, v95\n" \
  "s_add_u32 s89, s73, s88\n" \
  "s_waitcnt lgkmcnt(0)\n" \
  "buffer_store_dwordx4 v[68:71], v94, s[52:55], s89 offen sc1\n" \
  "s_cmp_eq_u32 s77, 1\n" \
  "s_cbranch_scc0 L_PE0_%=b\n" \
  "ds_read_b64_tr_b16 v[72:73], v35 offset:2048\n" \
  "ds_read_b64_tr_b16 v[74:75], v35 offset:2304\n" \
  "s_waitcnt lgkmcnt(0)\n" \
  "buffer_store_dwordx4 v[72:75], v94, s[52:55], s73 offen sc1\n" \
  "s_branch L_PE0_%=b\n" \
  "L_PV0_%=b:\n" \
  "v_lshrrev_b32 v77, 1, v33\n" \
  "s_nop 0\n" \
  "buffer_store_dwordx2 v[78:79], v77, s[52:55], s73 offen sc1\n" \
  "L_PE0_%=b:\n" \
  "s_cmp_eq_u32 s65, 0\n" \
  "s_cbranch_scc1 L_NORST0_%=\n" \
  "v_mov_b32 v72, -1\n" \
  "v_mov_b32 v73, -1\n" \
  "v_mov_b32 v74, -1\n" \
  "v_mov_b32 v75, -1\n" \
  "s_cmp_eq_u32 s77, 1\n" \
  "s_cbranch_scc0 L_H20_%=\n" \
  "s_add_u32 s89, s64, s71\n" \
  "buffer_store_dwordx4 v[72:75], v32, s[40:43], s89 offen offset:0 sc1\n" \
  "buffer_store_dwordx4 v[72:75], v32, s[40:43], s89 offen offset:1024 sc1\n" \
  "buffer_store_dwordx4 v[72:75], v32, s[40:43], s89 offen offset:2048 sc1\n" \
  "buffer_store_dwordx4 v[72:75], v32, s[40:43], s89 offen offset:3072 sc1\n" \
  "s_branch L_NORST0_%=\n" \
  "L_H20_%=:\n" \
  "s_cmp_eq_u32 s77, 2\n" \
  "s_cbranch_scc0 L_NORST0_%=\n" \
  "s_mov_b32 exec_lo, 0xff\n" \
  "s_mov_b32 exec_hi, 0xff\n" \
  "buffer_store_dword v72, v37, s[36:39], s69 offen sc1\n" \
  "s_add_u32 s89, s69, 16384\n" \
  "buffer_store_dword v72, v37, s[36:39], s89 offen sc1\n" \
  "s_mov_b64 exec, -1\n" \
  "L_NORST0_%=:\n" \
  "s_cmp_lt_u32 s65, 62\n" \
  "s_cbranch_scc0 L_NOK0_%=\n" \
  "s_add_u32 s91, s65, 2\n" \
  "s_lshr_b32 s91, s91, 1\n" \
  "s_add_u32 s91, s91, 1\n" \
  "s_cmp_ge_u32 s61, s91\n" \
  "s_cbranch_scc1 L_KVD0_%=\n" \
  "s_lshl_b32 s88, s80, 9\n" \
  "s_add_u32 s88, s88, 3840\n" \
  "v_and_b32 v77, 16, v32\n" \
  "v_lshlrev_b32 v77, 4, v77\n" \
  "v_add_u32 v77, s88, v77\n" \
  "L_KVC0_%=:\n" \
  "global_load_dword v94, v77, s[92:93] sc1\n" \
  "s_waitcnt vmcnt(0)\n" \
  "v_readlane_b32 s61, v94, 0\n" \
  "v_readlane_b32 s89, v94, 1\n" \
  "s_nop 1\n" \
  "s_min_u32 s61, s61, s89\n" \
  "s_cmp_ge_u32 s61, s91\n" \
  "s_cbranch_scc1 L_KVD0_%=\n" \
  "s_add_u32 s83, s83, 1\n" \
  "s_cmp_gt_u32 s83, 0x100000\n" \
  "s_cbranch_scc1 L_DEAD_%=\n" \
  "s_sleep 2\n" \
  "s_branch L_KVC0_%=\n" \
  "L_KVD0_%=:\n" \
  "L_NOK0_%=:\n" \
  "v_mov_b32 v40, 0\n" \
  "v_mov_b32 v41, 0\n" \
  "v_mov_b32 v42, 0\n" \
  "v_mov_b32 v43, 0\n" \
  "v_mov_b32 v44, 0\n" \
  "v_mov_b32 v45, 0\n" \
  "v_mov_b32 v46, 0\n" \
  "v_mov_b32 v47, 0\n" \
  "v_mov_b32 v48, 0\n" \
  "v_mov_b32 v49, 0\n" \
  "v_mov_b32 v50, 0\n" \
  "v_mov_b32 v51, 0\n" \
  "v_mov_b32 v52, 0\n" \
  "v_mov_b32 v53, 0\n" \
  "v_mov_b32 v54, 0\n" \
  "v_mov_b32 v55, 0\n" \
  "v_mov_b32 v56, 0\n" \
  "v_mov_b32 v57, 0\n" \
  "v_mov_b32 v58, 0\n" \
  "v_mov_b32 v59, 0\n" \
  "v_mov_b32 v60, 0\n" \
  "v_mov_b32 v61, 0\n" \
  "v_mov_b32 v62, 0\n" \
  "v_mov_b32 v63, 0\n" \
  "s_mov_b32 s88, s64\n" \
  "s_mov_b32 s64, s50\n" \
  "s_mov_b32 s50, s51\n" \
  "s_mov_b32 s51, s88\n" \
  "s_add_u32 s88, s65, 1\n" \
  "s_and_b32 s88, s88, 3\n" \
  "s_lshl_b32 s88, s88, 16\n" \
  "s_add_u32 s89, s88, s82\n" \
  "s_add_u32 s68, s89, s75\n" \
  "s_add_u32 s69, s66, s75\n" \
  "s_mov_b32 s66, s89\n" \
  "s_waitcnt vmcnt(0)\n" \
  "L_END0_%=:\n" \
  "s_add_u32 s65, s65, 1\n" \
  "L_TOP1_%=:\n" \
  "s_barrier\n" \
  "s_mov_b32 s91, s48\n" \
  "L_DL3_%=:\n" \
  "s_sub_u32 s91, s91, 1\n" \
  "s_cbranch_scc0 L_DL3_%=\n" \
  "L_ENTRY1_%=:\n" \
  "s_add_u32 s67, s66, s74\n" \
  "buffer_load_dwordx4 v[64:67], v32, s[36:39], s67 offen sc1\n" \
  "buffer_load_dwordx4 v[68:71], v32, s[36:39], s67 offen offset:1024 sc1\n" \
  "s_cmp_eq_u32 s77, 1\n" \
  "s_cbranch_scc1 L_QKW11_%=\n" \
  "s_cmp_eq_u32 s77, 2\n" \
  "s_cbranch_scc1 L_QKW21_%=\n" \
  "s_cmp_eq_u32 s77, 3\n" \
  "s_cbranch_scc1 L_QKW31_%=\n" \
  "L_QKW01_%=:\n" \
  "s_mov_b32 s59, 0\n" \
  "v_add_u32 v94, 0, v33\n" \
  "s_waitcnt vmcnt(0)\n" \
  "L_MOK01_%=:\n" \
  "ds_write_b128 v94, v[64:67] offset:40960\n" \
  "ds_write_b128 v94, v[68:71] offset:41984\n" \
  "v_max3_u32 v76, v64, v65, v66\n" \
  "v_max3_u32 v77, v67, v68, v69\n" \
  "v_max3_u32 v76, v70, v71, v76\n" \
  "v_max_u32 v76, v77, v76\n" \
  "v_cmp_eq_u32 vcc, -1, v76\n" \
  "s_cbranch_vccnz L_C101_%=\n" \
  "v_smfmac_f32_16x16x64_bf16 v[48:51], v[64:67], a[128:135], v39\n" \
  "v_smfmac_f32_16x16x64_bf16 v[52:55], v[64:67], a[192:199], v39\n" \
  "v_smfmac_f32_16x16x64_bf16 v[40:43], v[64:67], a[0:7], v39\n" \
  "v_smfmac_f32_16x16x64_bf16 v[44:47], v[64:67], a[64:71], v39\n" \
  "v_smfmac_f32_16x16x64_bf16 v[48:51], v[68:71], a[136:143], v39\n" \
  "v_smfmac_f32_16x16x64_bf16 v[52:55], v[68:71], a[200:207], v39\n" \
  "v_smfmac_f32_16x16x64_bf16 v[40:43], v[68:71], a[8:15], v39\n" \
  "v_smfmac_f32_16x16x64_bf16 v[44:47], v[68:71], a[72:79], v39\n" \
  "s_waitcnt lgkmcnt(0)\n" \
  "s_barrier\n" \
  "ds_read_b128 v[8:11], v32 offset:43008\n" \
  "ds_read_b128 v[12:15], v32 offset:44032\n" \
  "ds_read_b128 v[16:19], v32 offset:45056\n" \
  "ds_read_b128 v[20:23], v32 offset:46080\n" \
  "ds_read_b128 v[24:27], v32 offset:47104\n" \
  "ds_read_b128 v[28:31], v32 offset:48128\n" \
  "ds_read_b128 v[0:3], v32 offset:40960\n" \
  "ds_read_b128 v[4:7], v32 offset:41984\n" \
  "s_waitcnt lgkmcnt(7)\n" \
  "v_smfmac_f32_16x16x64_bf16 v[48:51], v[8:11], a[144:151], v39\n" \
  "v_smfmac_f32_16x16x64_bf16 v[52:55], v[8:11], a[208:215], v39\n" \
  "v_smfmac_f32_16x16x64_bf16 v[40:43], v[8:11], a[16:23], v39\n" \
  "v_smfmac_f32_16x16x64_bf16 v[44:47], v[8:11], a[80:87], v39\n" \
  "s_waitcnt lgkmcnt(6)\n" \
  "v_smfmac_f32_16x16x64_bf16 v[48:51], v[12:15], a[152:159], v39\n" \
  "v_smfmac_f32_16x16x64_bf16 v[52:55], v[12:15], a[216:223], v39\n" \
  "v_smfmac_f32_16x16x64_bf16 v[40:43], v[12:15], a[24:31], v39\n" \
  "v_smfmac_f32_16x16x64_bf16 v[44:47], v[12:15], a[88:95], v39\n" \
  "s_waitcnt lgkmcnt(5)\n" \
  "v_smfmac_f32_16x16x64_bf16 v[48:51], v[16:19], a[160:167], v39\n" \
  "v_smfmac_f32_16x16x64_bf16 v[52:55], v[16:19], a[224:231], v39\n" \
  "v_smfmac_f32_16x16x64_bf16 v[40:43], v[16:19], a[32:39], v39\n" \
  "v_smfmac_f32_16x16x64_bf16 v[44:47], v[16:19], a[96:103], v39\n" \
  "s_waitcnt lgkmcnt(4)\n" \
  "v_smfmac_f32_16x16x64_bf16 v[48:51], v[20:23], a[168:175], v39\n" \
  "v_smfmac_f32_16x16x64_bf16 v[52:55], v[20:23], a[232:239], v39\n" \
  "v_smfmac_f32_16x16x64_bf16 v[40:43], v[20:23], a[40:47], v39\n" \
  "v_smfmac_f32_16x16x64_bf16 v[44:47], v[20:23], a[104:111], v39\n" \
  "s_waitcnt lgkmcnt(3)\n" \
  "v_smfmac_f32_16x16x64_bf16 v[48:51], v[24:27], a[176:183], v39\n" \
  "v_smfmac_f32_16x16x64_bf16 v[52:55], v[24:27], a[240:247], v39\n" \
  "v_smfmac_f32_16x16x64_bf16 v[40:43], v[24:27], a[48:55], v39\n" \
  "v_smfmac_f32_16x16x64_bf16 v[44:47], v[24:27], a[112:119], v39\n" \
  "s_waitcnt lgkmcnt(2)\n" \
  "v_smfmac_f32_16x16x64_bf16 v[48:51], v[28:31], a[184:191], v39\n" \
  "v_smfmac_f32_16x16x64_bf16 v[52:55], v[28:31], a[248:255], v39\n" \
  "v_smfmac_f32_16x16x64_bf16 v[40:43], v[28:31], a[56:63], v39\n" \
  "v_smfmac_f32_16x16x64_bf16 v[44:47], v[28:31], a[120:127], v39\n" \
  "s_branch L_QKD1_%=\n" \
  ".p2align 6\n" \
  "L_QKW11_%=:\n" \
  "s_mov_b32 s59, 0\n" \
  "v_mov_b32 v77, s76\n" \
  "v_mov_b32 v94, s65\n" \
  "s_mov_b64 exec, 1\n" \
  "global_store_dword v77, v94, s[92:93] sc1\n" \
  "s_mov_b64 exec, -1\n" \
  "v_add_u32 v94, 1024, v33\n" \
  "s_waitcnt vmcnt(1)\n" \
  "L_MOK11_%=:\n" \
  "ds_write_b128 v94, v[64:67] offset:40960\n" \
  "ds_write_b128 v94, v[68:71] offset:41984\n" \
  "v_max3_u32 v76, v64, v65, v66\n" \
  "v_max3_u32 v77, v67, v68, v69\n" \
  "v_max3_u32 v76, v70, v71, v76\n" \
  "v_max_u32 v76, v77, v76\n" \
  "v_cmp_eq_u32 vcc, -1, v76\n" \
  "s_cbranch_vccnz L_C111_%=\n" \
  "v_smfmac_f32_16x16x64_bf16 v[48:51], v[64:67], a[144:151], v39\n" \
  "v_smfmac_f32_16x16x64_bf16 v[52:55], v[64:67], a[208:215], v39\n" \
  "v_smfmac_f32_16x16x64_bf16 v[40:43], v[64:67], a[16:23], v39\n" \
  "v_smfmac_f32_16x16x64_bf16 v[44:47], v[64:67], a[80:87], v39\n" \
  "v_smfmac_f32_16x16x64_bf16 v[48:51], v[68:71], a[152:159], v39\n" \
  "v_smfmac_f32_16x16x64_bf16 v[52:55], v[68:71], a[216:223], v39\n" \
  "v_smfmac_f32_16x16x64_bf16 v[40:43], v[68:71], a[24:31], v39\n" \
  "v_smfmac_f32_16x16x64_bf16 v[44:47], v[68:71], a[88:95], v39\n" \
  "s_waitcnt lgkmcnt(0)\n" \
  "s_barrier\n" \
  "ds_read_b128 v[0:3], v32 offset:40960\n" \
  "ds_read_b128 v[4:7], v32 offset:41984\n" \
  "ds_read_b128 v[16:19], v32 offset:45056\n" \
  "ds_read_b128 v[20:23], v32 offset:46080\n" \
  "ds_read_b128 v[24:27], v32 offset:47104\n" \
  "ds_read_b128 v[28:31], v32 offset:48128\n" \
  "ds_read_b128 v[8:11], v32 offset:43008\n" \
  "ds_read_b128 v[12:15], v32 offset:44032\n" \
  "s_waitcnt lgkmcnt(7)\n" \
  "v_smfmac_f32_16x16x64_bf16 v[48:51], v[0:3], a[128:135], v39\n" \
  "v_smfmac_f32_16x16x64_bf16 v[52:55], v[0:3], a[192:199], v39\n" \
  "s_cmp_lt_u32 s65, 63\n" \
  "v_smfmac_f32_16x16x64_bf16 v[40:43], v[0:3], a[0:7], v39\n" \
  "s_cselect_b32 s90, 0x80000, 0\n" \
  "v_smfmac_f32_16x16x64_bf16 v[44:47], v[0:3], a[64:71], v39\n" \
  "s_add_u32 s72, s72, s90\n" \
  "s_waitcnt lgkmcnt(6)\n" \
  "v_smfmac_f32_16x16x64_bf16 v[48:51], v[4:7], a[136:143], v39\n" \
  "s_mov_b32 m0, 8192\n" \
  "v_smfmac_f32_16x16x64_bf16 v[52:55], v[4:7], a[200:207], v39\n" \
  "s_add_u32 s89, s72, 0\n" \
  "v_smfmac_f32_16x16x64_bf16 v[40:43], v[4:7], a[8:15], v39\n" \
  "buffer_load_dwordx4 v32, s[44:47], s89 offen sc1 lds\n" \
  "v_smfmac_f32_16x16x64_bf16 v[44:47], v[4:7], a[72:79], v39\n" \
  "s_mov_b32 m0, 11264\n" \
  "s_waitcnt lgkmcnt(5)\n" \
  "v_smfmac_f32_16x16x64_bf16 v[48:51], v[16:19], a[160:167], v39\n" \
  "s_add_u32 s89, s72, 3072\n" \
  "v_smfmac_f32_16x16x64_bf16 v[52:55], v[16:19], a[224:231], v39\n" \
  "buffer_load_dwordx4 v32, s[44:47], s89 offen sc1 lds\n" \
  "v_smfmac_f32_16x16x64_bf16 v[40:43], v[16:19], a[32:39], v39\n" \
  "s_mov_b32 m0, 14336\n" \
  "v_smfmac_f32_16x16x64_bf16 v[44:47], v[16:19], a[96:103], v39\n" \
  "s_add_u32 s89, s72, 6144\n" \
  "s_waitcnt lgkmcnt(4)\n" \
  "v_smfmac_f32_16x16x64_bf16 v[48:51], v[20:23], a[168:175], v39\n" \
  "buffer_load_dwordx4 v32, s[44:47], s89 offen sc1 lds\n" \
  "v_smfmac_f32_16x16x64_bf16 v[52:55], v[20:23], a[232:239], v39\n" \
  "s_mov_b32 m0, 17408\n" \
  "v_smfmac_f32_16x16x64_bf16 v[40:43], v[20:23], a[40:47], v39\n" \
  "s_add_u32 s89, s72, 9216\n" \
  "v_smfmac_f32_16x16x64_bf16 v[44:47], v[20:23], a[104:111], v39\n" \
  "buffer_load_dwordx4 v32, s[44:47], s89 offen sc1 lds\n" \
  "s_waitcnt lgkmcnt(3)\n" \
  "v_smfmac_f32_16x16x64_bf16 v[48:51], v[24:27], a[176:183], v39\n" \
  "s_mov_b32 m0, 20480\n" \
  "v_smfmac_f32_16x16x64_bf16 v[52:55], v[24:27], a[240:247], v39\n" \
  "s_add_u32 s89, s72, 12288\n" \
  "v_smfmac_f32_16x16x64_bf16 v[40:43], v[24:27], a[48:55], v39\n" \
  "buffer_load_dwordx4 v32, s[44:47], s89 offen sc1 lds\n" \
  "v_smfmac_f32_16x16x64_bf16 v[44:47], v[24:27], a[112:119], v39\n" \
  "s_mov_b32 m0, 23552\n" \
  "s_waitcnt lgkmcnt(2)\n" \
  "v_smfmac_f32_16x16x64_bf16 v[48:51], v[28:31], a[184:191], v39\n" \
  "s_add_u32 s89, s72, 15360\n" \
  "v_smfmac_f32_16x16x64_bf16 v[52:55], v[28:31], a[248:255], v39\n" \
  "buffer_load_dwordx4 v32, s[44:47], s89 offen sc1 lds\n" \
  "v_smfmac_f32_16x16x64_bf16 v[40:43], v[28:31], a[56:63], v39\n" \
  "v_smfmac_f32_16x16x64_bf16 v[44:47], v[28:31], a[120:127], v39\n" \
  "s_branch L_QKD1_%=\n" \
  ".p2align 6\n" \
  "L_QKW21_%=:\n" \
  "s_mov_b32 s59, 0\n" \
  "v_add_u32 v94, 2048, v33\n" \
  "s_waitcnt vmcnt(0)\n" \
  "L_MOK21_%=:\n" \
  "ds_write_b128 v94, v[64:67] offset:40960\n" \
  "ds_write_b128 v94, v[68:71] offset:41984\n" \
  "v_max3_u32 v76, v64, v65, v66\n" \
  "v_max3_u32 v77, v67, v68, v69\n" \
  "v_max3_u32 v76, v70, v71, v76\n" \
  "v_max_u32 v76, v77, v76\n" \
  "v_cmp_eq_u32 vcc, -1, v76\n" \
  "s_cbranch_vccnz L_C121_%=\n" \
  "v_smfmac_f32_16x16x64_bf16 v[48:51], v[64:67], a[160:167], v39\n" \
  "v_smfmac_f32_16x16x64_bf16 v[52:55], v[64:67], a[224:231], v39\n" \
  "v_smfmac_f32_16x16x64_bf16 v[40:43], v[64:67], a[32:39], v39\n" \
  "v_smfmac_f32_16x16x64_bf16 v[44:47], v[64:67], a[96:103], v39\n" \
  "v_smfmac_f32_16x16x64_bf16 v[48:51], v[68:71], a[168:175], v39\n" \
  "v_smfmac_f32_16x16x64_bf16 v[52:55], v[68:71], a[232:239], v39\n" \
  "v_smfmac_f32_16x16x64_bf16 v[40:43], v[68:71], a[40:47], v39\n" \
  "v_smfmac_f32_16x16x64_bf16 v[44:47], v[68:71], a[104:111], v39\n" \
  "s_waitcnt lgkmcnt(0)\n" \
  "s_barrier\n" \
  "ds_read_b128 v[0:3], v32 offset:40960\n" \
  "ds_read_b128 v[4:7], v32 offset:41984\n" \
  "ds_read_b128 v[8:11], v32 offset:43008\n" \
  "ds_read_b128 v[12:15], v32 offset:44032\n" \
  "ds_read_b128 v[24:27], v32 offset:47104\n" \
  "ds_read_b128 v[28:31], v32 offset:48128\n" \
  "ds_read_b128 v[16:19], v32 offset:45056\n" \
  "ds_read_b128 v[20:23], v32 offset:46080\n" \
  "s_waitcnt lgkmcnt(7)\n" \
  "v_smfmac_f32_16x16x64_bf16 v[48:51], v[0:3], a[128:135], v39\n" \
  "v_smfmac_f32_16x16x64_bf16 v[52:55], v[0:3], a[192:199], v39\n" \
  "s_cmp_lt_u32 s65, 63\n" \
  "v_smfmac_f32_16x16x64_bf16 v[40:43], v[0:3], a[0:7], v39\n" \
  "s_cselect_b32 s90, 0x80000, 0\n" \
  "v_smfmac_f32_16x16x64_bf16 v[44:47], v[0:3], a[64:71], v39\n" \
  "s_add_u32 s72, s72, s90\n" \
  "s_waitcnt lgkmcnt(6)\n" \
  "v_smfmac_f32_16x16x64_bf16 v[48:51], v[4:7], a[136:143], v39\n" \
  "s_mov_b32 m0, 9216\n" \
  "v_smfmac_f32_16x16x64_bf16 v[52:55], v[4:7], a[200:207], v39\n" \
  "s_add_u32 s89, s72, 1024\n" \
  "v_smfmac_f32_16x16x64_bf16 v[40:43], v[4:7], a[8:15], v39\n" \
  "buffer_load_dwordx4 v32, s[44:47], s89 offen sc1 lds\n" \
  "v_smfmac_f32_16x16x64_bf16 v[44:47], v[4:7], a[72:79], v39\n" \
  "s_mov_b32 m0, 12288\n" \
  "s_waitcnt lgkmcnt(5)\n" \
  "v_smfmac_f32_16x16x64_bf16 v[48:51], v[8:11], a[144:151], v39\n" \
  "s_add_u32 s89, s72, 4096\n" \
  "v_smfmac_f32_16x16x64_bf16 v[52:55], v[8:11], a[208:215], v39\n" \
  "buffer_load_dwordx4 v32, s[44:47], s89 offen sc1 lds\n" \
  "v_smfmac_f32_16x16x64_bf16 v[40:43], v[8:11], a[16:23], v39\n" \
  "s_mov_b32 m0, 15360\n" \
  "v_smfmac_f32_16x16x64_bf16 v[44:47], v[8:11], a[80:87], v39\n" \
  "s_add_u32 s89, s72, 7168\n" \
  "s_waitcnt lgkmcnt(4)\n" \
  "v_smfmac_f32_16x16x64_bf16 v[48:51], v[12:15], a[152:159], v39\n" \
  "buffer_load_dwordx4 v32, s[44:47], s89 offen sc1 lds\n" \
  "v_smfmac_f32_16x16x64_bf16 v[52:55], v[12:15], a[216:223], v39\n" \
  "s_mov_b32 m0, 18432\n" \
  "v_smfmac_f32_16x16x64_bf16 v[40:43], v[12:15], a[24:31], v39\n" \
  "s_add_u32 s89, s72, 10240\n" \
  "v_smfmac_f32_16x16x64_bf16 v[44:47], v[12:15], a[88:95], v39\n" \
  "buffer_load_dwordx4 v32, s[44:47], s89 offen sc1 lds\n" \
  "s_waitcnt lgkmcnt(3)\n" \
  "v_smfmac_f32_16x16x64_bf16 v[48:51], v[24:27], a[176:183], v39\n" \
  "s_mov_b32 m0, 21504\n" \
  "v_smfmac_f32_16x16x64_bf16 v[52:55], v[24:27], a[240:247], v39\n" \
  "s_add_u32 s89, s72, 13312\n" \
  "v_smfmac_f32_16x16x64_bf16 v[40:43], v[24:27], a[48:55], v39\n" \
  "buffer_load_dwordx4 v32, s[44:47], s89 offen sc1 lds\n" \
  "v_smfmac_f32_16x16x64_bf16 v[44:47], v[24:27], a[112:119], v39\n" \
  "s_waitcnt lgkmcnt(2)\n" \
  "v_smfmac_f32_16x16x64_bf16 v[48:51], v[28:31], a[184:191], v39\n" \
  "v_smfmac_f32_16x16x64_bf16 v[52:55], v[28:31], a[248:255], v39\n" \
  "v_smfmac_f32_16x16x64_bf16 v[40:43], v[28:31], a[56:63], v39\n" \
  "v_smfmac_f32_16x16x64_bf16 v[44:47], v[28:31], a[120:127], v39\n" \
  "s_branch L_QKD1_%=\n" \
  ".p2align 6\n" \
  "L_QKW31_%=:\n" \
  "s_mov_b32 s59, 0\n" \
  "v_add_u32 v94, 3072, v33\n" \
  "s_waitcnt vmcnt(0)\n" \
  "L_MOK31_%=:\n" \
  "ds_write_b128 v94, v[64:67] offset:40960\n" \
  "ds_write_b128 v94, v[68:71] offset:41984\n" \
  "v_max3_u32 v76, v64, v65, v66\n" \
  "v_max3_u32 v77, v67, v68, v69\n" \
  "v_max3_u32 v76, v70, v71, v76\n" \
  "v_max_u32 v76, v77, v76\n" \
  "v_cmp_eq_u32 vcc, -1, v76\n" \
  "s_cbranch_vccnz L_C131_%=\n" \
  "v_smfmac_f32_16x16x64_bf16 v[48:51], v[64:67], a[176:183], v39\n" \
  "v_smfmac_f32_16x16x64_bf16 v[52:55], v[64:67], a[240:247], v39\n" \
  "v_smfmac_f32_16x16x64_bf16 v[40:43], v[64:67], a[48:55], v39\n" \
  "v_smfmac_f32_16x16x64_bf16 v[44:47], v[64:67], a[112:119], v39\n" \
  "v_smfmac_f32_16x16x64_bf16 v[48:51], v[68:71], a[184:191], v39\n" \
  "v_smfmac_f32_16x16x64_bf16 v[52:55], v[68:71], a[248:255], v39\n" \
  "v_smfmac_f32_16x16x64_bf16 v[40:43], v[68:71], a[56:63], v39\n" \
  "v_smfmac_f32_16x16x64_bf16 v[44:47], v[68:71], a[120:127], v39\n" \
  "s_waitcnt lgkmcnt(0)\n" \
  "s_barrier\n" \
  "ds_read_b128 v[0:3], v32 offset:40960\n" \
  "ds_read_b128 v[4:7], v32 offset:41984\n" \
  "ds_read_b128 v[8:11], v32 offset:43008\n" \
  "ds_read_b128 v[12:15], v32 offset:44032\n" \
  "ds_read_b128 v[16:19], v32 offset:45056\n" \
  "ds_read_b128 v[20:23], v32 offset:46080\n" \
  "ds_read_b128 v[24:27], v32 offset:47104\n" \
  "ds_read_b128 v[28:31], v32 offset:48128\n" \
  "s_waitcnt lgkmcnt(7)\n" \
  "v_smfmac_f32_16x16x64_bf16 v[48:51], v[0:3], a[128:135], v39\n" \
  "v_smfmac_f32_16x16x64_bf16 v[52:55], v[0:3], a[192:199], v39\n" \
  "s_cmp_lt_u32 s65, 63\n" \
  "v_smfmac_f32_16x16x64_bf16 v[40:43], v[0:3], a[0:7], v39\n" \
  "s_cselect_b32 s90, 0x80000, 0\n" \
  "v_smfmac_f32_16x16x64_bf16 v[44:47], v[0:3], a[64:71], v39\n" \
  "s_add_u32 s72, s72, s90\n" \
  "s_waitcnt lgkmcnt(6)\n" \
  "v_smfmac_f32_16x16x64_bf16 v[48:51], v[4:7], a[136:143], v39\n" \
  "s_mov_b32 m0, 10240\n" \
  "v_smfmac_f32_16x16x64_bf16 v[52:55], v[4:7], a[200:207], v39\n" \
  "s_add_u32 s89, s72, 2048\n" \
  "v_smfmac_f32_16x16x64_bf16 v[40:43], v[4:7], a[8:15], v39\n" \
  "buffer_load_dwordx4 v32, s[44:47], s89 offen sc1 lds\n" \
  "v_smfmac_f32_16x16x64_bf16 v[44:47], v[4:7], a[72:79], v39\n" \
  "s_mov_b32 m0, 13312\n" \
  "s_waitcnt lgkmcnt(5)\n" \
  "v_smfmac_f32_16x16x64_bf16 v[48:51], v[8:11], a[144:151], v39\n" \
  "s_add_u32 s89, s72, 5120\n" \
  "v_smfmac_f32_16x16x64_bf16 v[52:55], v[8:11], a[208:215], v39\n" \
  "buffer_load_dwordx4 v32, s[44:47], s89 offen sc1 lds\n" \
  "v_smfmac_f32_16x16x64_bf16 v[40:43], v[8:11], a[16:23], v39\n" \
  "s_mov_b32 m0, 16384\n" \
  "v_smfmac_f32_16x16x64_bf16 v[44:47], v[8:11], a[80:87], v39\n" \
  "s_add_u32 s89, s72, 8192\n" \
  "s_waitcnt lgkmcnt(4)\n" \
  "v_smfmac_f32_16x16x64_bf16 v[48:51], v[12:15], a[152:159], v39\n" \
  "buffer_load_dwordx4 v32, s[44:47], s89 offen sc1 lds\n" \
  "v_smfmac_f32_16x16x64_bf16 v[52:55], v[12:15], a[216:223], v39\n" \
  "s_mov_b32 m0, 19456\n" \
  "v_smfmac_f32_16x16x64_bf16 v[40:43], v[12:15], a[24:31], v39\n" \
  "s_add_u32 s89, s72, 11264\n" \
  "v_smfmac_f32_16x16x64_bf16 v[44:47], v[12:15], a[88:95], v39\n" \
  "buffer_load_dwordx4 v32, s[44:47], s89 offen sc1 lds\n" \
  "s_waitcnt lgkmcnt(3)\n" \
  "v_smfmac_f32_16x16x64_bf16 v[48:51], v[16:19], a[160:167], v39\n" \
  "s_mov_b32 m0, 22528\n" \
  "v_smfmac_f32_16x16x64_bf16 v[52:55], v[16:19], a[224:231], v39\n" \
  "s_add_u32 s89, s72, 14336\n" \
  "v_smfmac_f32_16x16x64_bf16 v[40:43], v[16:19], a[32:39], v39\n" \
  "buffer_load_dwordx4 v32, s[44:47], s89 offen sc1 lds\n" \
  "v_smfmac_f32_16x16x64_bf16 v[44:47], v[16:19], a[96:103], v39\n" \
  "s_waitcnt lgkmcnt(2)\n" \
  "v_smfmac_f32_16x16x64_bf16 v[48:51], v[20:23], a[168:175], v39\n" \
  "v_smfmac_f32_16x16x64_bf16 v[52:55], v[20:23], a[232:239], v39\n" \
  "v_smfmac_f32_16x16x64_bf16 v[40:43], v[20:23], a[40:47], v39\n" \
  "v_smfmac_f32_16x16x64_bf16 v[44:47], v[20:23], a[104:111], v39\n" \
  "s_branch L_QKD1_%=\n" \
  ".p2align 6\n" \
  "L_QKD1_%=:\n" \
  "s_waitcnt lgkmcnt(0)\n" \
  "v_smfmac_f32_16x16x64_bf16 v[56:59], v[0:3], v[128:135], v39\n" \
  "v_smfmac_f32_16x16x64_bf16 v[60:63], v[0:3], v[192:199], v39\n" \
  "s_nop 4\n" \
  "v_permlane32_swap_b32 v48, v52\n" \
  "v_permlane32_swap_b32 v49, v53\n" \
  "v_permlane32_swap_b32 v50, v54\n" \
  "v_permlane32_swap_b32 v51, v55\n" \
  "v_smfmac_f32_16x16x64_bf16 v[56:59], v[4:7], v[136:143], v39\n" \
  "v_pk_add_f32 v[48:49], v[48:49], v[52:53]\n" \
  "v_pk_add_f32 v[50:51], v[50:51], v[54:55]\n" \
  "v_permlane32_swap_b32 v40, v44\n" \
  "v_permlane32_swap_b32 v41, v45\n" \
  "v_cvt_pk_bf16_f32 v52, v48, v49\n" \
  "v_cvt_pk_bf16_f32 v53, v50, v51\n" \
  "v_smfmac_f32_16x16x64_bf16 v[60:63], v[4:7], v[200:207], v39\n" \
  "v_permlane32_swap_b32 v42, v46\n" \
  "v_permlane32_swap_b32 v43, v47\n" \
  "ds_write_b64 v34, v[52:53] offset:2048\n" \
  "v_pk_add_f32 v[40:41], v[40:41], v[44:45]\n" \
  "v_pk_add_f32 v[42:43], v[42:43], v[46:47]\n" \
  "v_cvt_pk_bf16_f32 v44, v40, v41\n" \
  "v_cvt_pk_bf16_f32 v45, v42, v43\n" \
  "ds_write_b64 v34, v[44:45] offset:0\n" \
  "v_smfmac_f32_16x16x64_bf16 v[56:59], v[8:11], v[144:151], v39\n" \
  "v_smfmac_f32_16x16x64_bf16 v[60:63], v[8:11], v[208:215], v39\n" \
  "v_smfmac_f32_16x16x64_bf16 v[56:59], v[12:15], v[152:159], v39\n" \
  "v_smfmac_f32_16x16x64_bf16 v[60:63], v[12:15], v[216:223], v39\n" \
  "s_waitcnt lgkmcnt(0)\n" \
  "s_barrier\n" \
  "ds_read_b64_tr_b16 v[40:41], v35 offset:0\n" \
  "ds_read_b64_tr_b16 v[42:43], v35 offset:256\n" \
  "v_smfmac_f32_16x16x64_bf16 v[56:59], v[16:19], v[160:167], v39\n" \
  "ds_read_b64_tr_b16 v[44:45], v35 offset:2048\n" \
  "ds_read_b64_tr_b16 v[46:47], v35 offset:2304\n" \
  "v_smfmac_f32_16x16x64_bf16 v[60:63], v[16:19], v[224:231], v39\n" \
  "ds_read_b128 v[0:3], v32 offset:24576\n" \
  "ds_read_b128 v[4:7], v32 offset:28672\n" \
  "v_smfmac_f32_16x16x64_bf16 v[56:59], v[20:23], v[168:175], v39\n" \
  "ds_read_b64_tr_b16 v[48:49], v35 offset:512\n" \
  "ds_read_b64_tr_b16 v[50:51], v35 offset:768\n" \
  "v_smfmac_f32_16x16x64_bf16 v[60:63], v[20:23], v[232:239], v39\n" \
  "ds_read_b64_tr_b16 v[52:53], v35 offset:2560\n" \
  "ds_read_b64_tr_b16 v[54:55], v35 offset:2816\n" \
  "v_smfmac_f32_16x16x64_bf16 v[56:59], v[24:27], v[176:183], v39\n" \
  "ds_read_b128 v[8:11], v32 offset:25600\n" \
  "ds_read_b128 v[12:15], v32 offset:29696\n" \
  "v_smfmac_f32_16x16x64_bf16 v[60:63], v[24:27], v[240:247], v39\n" \
  "v_smfmac_f32_16x16x64_bf16 v[56:59], v[28:31], v[184:191], v39\n" \
  "v_smfmac_f32_16x16x64_bf16 v[60:63], v[28:31], v[248:255], v39\n" \
  "s_waitcnt lgkmcnt(6)\n" \
  "v_mfma_f32_16x16x32_bf16 v[64:67], v[44:47], v[40:43], 0\n" \
  "ds_read_b64_tr_b16 v[44:45], v35 offset:3072\n" \
  "ds_read_b64_tr_b16 v[46:47], v35 offset:3328\n" \
  "v_mfma_f32_16x16x32_bf16 v[68:71], v[0:3], v[40:43], 0\n" \
  "ds_read_b128 v[16:19], v32 offset:26624\n" \
  "ds_read_b128 v[20:23], v32 offset:30720\n" \
  "v_mfma_f32_16x16x32_bf16 v[72:75], v[4:7], v[40:43], 0\n" \
  "ds_read_b64_tr_b16 v[40:41], v35 offset:1024\n" \
  "ds_read_b64_tr_b16 v[42:43], v35 offset:1280\n" \
  "s_waitcnt lgkmcnt(6)\n" \
  "v_mfma_f32_16x16x32_bf16 v[64:67], v[52:55], v[48:51], v[64:67]\n" \
  "ds_read_b64_tr_b16 v[52:53], v35 offset:3584\n" \
  "ds_read_b64_tr_b16 v[54:55], v35 offset:3840\n" \
  "v_mfma_f32_16x16x32_bf16 v[68:71], v[8:11], v[48:51], v[68:71]\n" \
  "ds_read_b128 v[24:27], v32 offset:27648\n" \
  "ds_read_b128 v[28:31], v32 offset:31744\n" \
  "v_mfma_f32_16x16x32_bf16 v[72:75], v[12:15], v[48:51], v[72:75]\n" \
  "ds_read_b64_tr_b16 v[48:49], v35 offset:1536\n" \
  "ds_read_b64_tr_b16 v[50:51], v35 offset:1792\n" \
  "ds_read_b128 v[0:3], v33 offset:32768\n" \
  "ds_read_b128 v[4:7], v33 offset:36864\n" \
  "s_waitcnt lgkmcnt(8)\n" \
  "v_mfma_f32_16x16x32_bf16 v[64:67], v[44:47], v[40:43], v[64:67]\n" \
  "v_permlane32_swap_b32 v56, v60\n" \
  "v_mfma_f32_16x16x32_bf16 v[68:71], v[16:19], v[40:43], v[68:71]\n" \
  "v_permlane32_swap_b32 v57, v61\n" \
  "v_mfma_f32_16x16x32_bf16 v[72:75], v[20:23], v[40:43], v[72:75]\n" \
  "v_permlane32_swap_b32 v58, v62\n" \
  "s_waitcnt lgkmcnt(2)\n" \
  "v_mfma_f32_16x16x32_bf16 v[64:67], v[52:55], v[48:51], v[64:67]\n" \
  "v_permlane32_swap_b32 v59, v63\n" \
  "v_mfma_f32_16x16x32_bf16 v[68:71], v[24:27], v[48:51], v[68:71]\n" \
  "v_pk_add_f32 v[56:57], v[56:57], v[60:61]\n" \
  "v_mfma_f32_16x16x32_bf16 v[72:75], v[28:31], v[48:51], v[72:75]\n" \
  "v_pk_add_f32 v[58:59], v[58:59], v[62:63]\n" \
  "s_cmp_eq_u32 s65, 63\n" \
  "v_cvt_pk_bf16_f32 v78, v56, v57\n" \
  "v_cvt_pk_bf16_f32 v79, v58, v59\n" \
  "s_cbranch_scc1 L_LAST_%=\n" \
  "s_add_u32 s70, s50, s71\n" \
  "s_nop 1\n" \
  "v_max3_f32 v77, v64, v65, v66\n" \
  "v_max3_f32 v94, v67, v68, v69\n" \
  "v_max3_f32 v95, v70, v71, v72\n" \
  "v_max3_f32 v77, v77, v73, v74\n" \
  "v_max3_f32 v94, v94, v75, v95\n" \
  "v_max_f32 v95, v77, v94\n" \
  "v_max_f32 v77, v77, v94\n" \
  "s_nop 1\n" \
  "v_permlane16_swap_b32 v77, v95\n" \
  "v_max_f32 v94, v77, v95\n" \
  "v_max_f32 v77, v77, v95\n" \
  "s_nop 1\n" \
  "v_permlane32_swap_b32 v77, v94\n" \
  "v_max_f32 v77, v77, v94\n" \
  "v_mul_f32 v94, s58, v77\n" \
  "v_pk_fma_f32 v[64:65], v[64:65], s[56:57], v[94:95] op_sel_hi:[1,1,0]\n" \
  "v_pk_fma_f32 v[66:67], v[66:67], s[56:57], v[94:95] op_sel_hi:[1,1,0]\n" \
  "v_pk_fma_f32 v[68:69], v[68:69], s[56:57], v[94:95] op_sel_hi:[1,1,0]\n" \
  "v_pk_fma_f32 v[70:71], v[70:71], s[56:57], v[94:95] op_sel_hi:[1,1,0]\n" \
  "v_pk_fma_f32 v[72:73], v[72:73], s[56:57], v[94:95] op_sel_hi:[1,1,0]\n" \
  "v_pk_fma_f32 v[74:75], v[74:75], s[56:57], v[94:95] op_sel_hi:[1,1,0]\n" \
  "v_exp_f32 v64, v64\n" \
  "v_exp_f32 v65, v65\n" \
  "v_exp_f32 v66, v66\n" \
  "v_exp_f32 v67, v67\n" \
  "v_exp_f32 v68, v68\n" \
  "v_cvt_pk_bf16_f32 v94, v64, v65\n" \
  "v_cvt_pk_bf16_f32 v95, v66, v67\n" \
  "v_exp_f32 v69, v69\n" \
  "v_cndmask_b32 v82, 0, v94, s[84:85]\n" \
  "v_cndmask_b32 v83, 0, v95, s[84:85]\n" \
  "v_cndmask_b32 v86, v94, 0, s[84:85]\n" \
  "v_cndmask_b32 v87, v95, 0, s[84:85]\n" \
  "v_exp_f32 v70, v70\n" \
  "v_mfma_f32_16x16x32_bf16 v[44:47], v[78:81], v[82:85], 0\n" \
  "v_exp_f32 v71, v71\n" \
  "v_exp_f32 v72, v72\n" \
  "v_mfma_f32_16x16x32_bf16 v[48:51], v[78:81], v[86:89], 0\n" \
  "v_exp_f32 v73, v73\n" \
  "v_exp_f32 v74, v74\n" \
  "v_mfma_f32_16x16x32_bf16 v[56:59], v[90:93], v[82:85], 0\n" \
  "v_exp_f32 v75, v75\n" \
  "v_cvt_pk_bf16_f32 v40, v68, v69\n" \
  "v_cvt_pk_bf16_f32 v41, v70, v71\n" \
  "v_cvt_pk_bf16_f32 v42, v72, v73\n" \
  "v_cvt_pk_bf16_f32 v43, v74, v75\n" \
  "s_waitcnt lgkmcnt(0)\n" \
  "s_nop 0\n" \
  "v_mfma_f32_16x16x32_bf16 v[44:47], v[0:3], v[40:43], v[44:47]\n" \
  "v_mfma_f32_16x16x32_bf16 v[48:51], v[4:7], v[40:43], v[48:51]\n" \
  "v_mfma_f32_16x16x32_bf16 v[56:59], v[90:93], v[40:43], v[56:59]\n" \
  "s_nop 5\n" \
  "v_cvt_pk_bf16_f32 v52, v44, v45\n" \
  "v_cvt_pk_bf16_f32 v53, v46, v47\n" \
  "v_cvt_pk_bf16_f32 v54, v48, v49\n" \
  "v_cvt_pk_bf16_f32 v55, v50, v51\n" \
  "ds_write_b128 v33, v[52:55] offset:4096\n" \
  "v_rcp_f32 v77, v56\n" \
  "s_waitcnt lgkmcnt(0)\n" \
  "s_barrier\n" \
  "ds_read_b128 v[8:11], v32 offset:4096\n" \
  "ds_read_b128 v[12:15], v32 offset:5120\n" \
  "ds_read_b128 v[16:19], v32 offset:6144\n" \
  "ds_read_b128 v[20:23], v32 offset:7168\n" \
  "s_waitcnt lgkmcnt(3)\n" \
  "v_mfma_f32_16x16x32_bf16 v[56:59], v[96:99], v[8:11], 0\n" \
  "v_mfma_f32_16x16x32_bf16 v[60:63], v[112:115], v[8:11], 0\n" \
  "s_waitcnt lgkmcnt(2)\n" \
  "v_mfma_f32_16x16x32_bf16 v[56:59], v[100:103], v[12:15], v[56:59]\n" \
  "v_mfma_f32_16x16x32_bf16 v[60:63], v[116:119], v[12:15], v[60:63]\n" \
  "s_waitcnt lgkmcnt(1)\n" \
  "v_mfma_f32_16x16x32_bf16 v[56:59], v[104:107], v[16:19], v[56:59]\n" \
  "v_mfma_f32_16x16x32_bf16 v[60:63], v[120:123], v[16:19], v[60:63]\n" \
  "s_waitcnt lgkmcnt(0)\n" \
  "v_mfma_f32_16x16x32_bf16 v[56:59], v[108:111], v[20:23], v[56:59]\n" \
  "v_mfma_f32_16x16x32_bf16 v[60:63], v[124:127], v[20:23], v[60:63]\n" \
  "s_nop 7\n" \
  "v_cndmask_b32 v64, v56, v60, s[86:87]\n" \
  "v_cndmask_b32 v65, v57, v61, s[86:87]\n" \
  "v_cndmask_b32 v66, v58, v62, s[86:87]\n" \
  "v_cndmask_b32 v67, v59, v63, s[86:87]\n" \
  "v_pk_mul_f32 v[64:65], v[64:65], v[76:77] op_sel:[0,1] op_sel_hi:[1,1]\n" \
  "v_pk_mul_f32 v[66:67], v[66:67], v[76:77] op_sel:[0,1] op_sel_hi:[1,1]\n" \
  "buffer_store_dwordx4 v[64:67], v36, s[40:43], s50 offen sc1\n" \
  "s_cmp_eq_u32 s77, 0\n" \
  "s_cbranch_scc0 L_HELP1_%=\n" \
  "s_lshl_b32 s88, s65, 5\n" \
  "s_add_u32 s88, s88, s80\n" \
  "s_lshl_b32 s73, s88, 11\n" \
  "s_cmp_eq_u32 s78, 0\n" \
  "s_cbranch_scc1 L_PE1_%=a\n" \
  "v_lshrrev_b32 v77, 1, v33\n" \
  "s_nop 0\n" \
  "buffer_store_dwordx2 v[78:79], v77, s[52:55], s73 offen sc1\n" \
  "L_PE1_%=a:\n" \
  "v_mov_b32 v40, 0\n" \
  "v_mov_b32 v41, 0\n" \
  "v_mov_b32 v42, 0\n" \
  "v_mov_b32 v43, 0\n" \
  "v_mov_b32 v44, 0\n" \
  "v_mov_b32 v45, 0\n" \
  "v_mov_b32 v46, 0\n" \
  "v_mov_b32 v47, 0\n" \
  "v_mov_b32 v48, 0\n" \
  "v_mov_b32 v49, 0\n" \
  "v_mov_b32 v50, 0\n" \
  "v_mov_b32 v51, 0\n" \
  "v_mov_b32 v52, 0\n" \
  "v_mov_b32 v53, 0\n" \
  "v_mov_b32 v54, 0\n" \
  "v_mov_b32 v55, 0\n" \
  "v_mov_b32 v56, 0\n" \
  "v_mov_b32 v57, 0\n" \
  "v_mov_b32 v58, 0\n" \
  "v_mov_b32 v59, 0\n" \
  "v_mov_b32 v60, 0\n" \
  "v_mov_b32 v61, 0\n" \
  "v_mov_b32 v62, 0\n" \
  "v_mov_b32 v63, 0\n" \
  "s_mov_b32 s88, s64\n" \
  "s_mov_b32 s64, s50\n" \
  "s_mov_b32 s50, s51\n" \
  "s_mov_b32 s51, s88\n" \
  "s_add_u32 s88, s65, 1\n" \
  "s_and_b32 s88, s88, 3\n" \
  "s_lshl_b32 s88, s88, 16\n" \
  "s_add_u32 s89, s88, s82\n" \
  "s_add_u32 s68, s89, s75\n" \
  "s_add_u32 s69, s66, s75\n" \
  "s_mov_b32 s66, s89\n" \
  "s_mov_b32 s91, s49\n" \
  "L_DL4_%=:\n" \
  "s_sub_u32 s91, s91, 1\n" \
  "s_cbranch_scc0 L_DL4_%=\n" \
  "L_PPOLL1_%=:\n" \
  "buffer_load_dwordx4 v[0:3], v32, s[40:43], s70 offen offset:0 sc1\n" \
  "buffer_load_dwordx4 v[4:7], v32, s[40:43], s70 offen offset:1024 sc1\n" \
  "buffer_load_dwordx4 v[8:11], v32, s[40:43], s70 offen offset:2048 sc1\n" \
  "buffer_load_dwordx4 v[12:15], v32, s[40:43], s70 offen offset:3072 sc1\n" \
  "s_waitcnt vmcnt(2)\n" \
  "v_max3_u32 v77, v0, v1, v2\n" \
  "v_max3_u32 v94, v3, v4, v5\n" \
  "v_pk_add_f32 v[16:17], v[0:1], v[4:5]\n" \
  "v_max3_u32 v77, v6, v7, v77\n" \
  "v_pk_add_f32 v[18:19], v[2:3], v[6:7]\n" \
  "s_waitcnt vmcnt(0)\n" \
  "v_max3_u32 v94, v8, v9, v94\n" \
  "v_max3_u32 v77, v10, v11, v77\n" \
  "v_pk_add_f32 v[20:21], v[8:9], v[12:13]\n" \
  "v_max3_u32 v94, v12, v13, v94\n" \
  "v_max3_u32 v77, v14, v15, v77\n" \
  "v_pk_add_f32 v[22:23], v[10:11], v[14:15]\n" \
  "v_max_u32 v77, v77, v94\n" \
  "v_pk_add_f32 v[16:17], v[16:17], v[20:21]\n" \
  "v_cmp_eq_u32 vcc, -1, v77\n" \
  "v_pk_add_f32 v[18:19], v[18:19], v[22:23]\n" \
  "s_cbranch_vccnz L_RETRYP1_%=\n" \
  "s_nop 0\n" \
  "v_permlane16_swap_b32 v16, v17\n" \
  "v_permlane16_swap_b32 v18, v19\n" \
  "v_add_f32 v16, v16, v17\n" \
  "v_add_f32 v18, v18, v19\n" \
  "s_nop 1\n" \
  "v_permlane32_swap_b32 v16, v18\n" \
  "v_add_f32 v16, v16, v18\n" \
  "s_nop 1\n" \
  "v_add_f32_dpp v16, v16, v16 row_ror:8 row_mask:0xf bank_mask:0xf\n" \
  "v_mov_b32 v17, v16\n" \
  "s_nop 1\n" \
  "v_permlane16_swap_b32 v16, v17\n" \
  "s_nop 0\n" \
  "v_cvt_pk_bf16_f32 v16, v16, v17\n" \
  "v_min_u32 v16, -2, v16\n" \
  "s_mov_b32 exec_lo, 0xff\n" \
  "s_mov_b32 exec_hi, 0xff\n" \
  "buffer_store_dword v16, v37, s[36:39], s68 offen sc1\n" \
  "s_add_u32 s89, s68, 16384\n" \
  "buffer_store_dword v16, v37, s[36:39], s89 offen sc1\n" \
  "s_mov_b64 exec, -1\n" \
  "s_branch L_END1_%=\n" \
  ".p2align 6\n" \
  "L_HELP1_%=:\n" \
  "s_lshl_b32 s88, s65, 5\n" \
  "s_add_u32 s88, s88, s80\n" \
  "s_lshl_b32 s73, s88, 11\n" \
  "s_cmp_eq_u32 s78, 0\n" \
  "s_cbranch_scc0 L_PV1_%=b\n" \
  "s_lshl_b32 s88, s77, 9\n" \
  "v_add_u32 v77, s88, v35\n" \
  "ds_read_b64_tr_b16 v[68:69], v77 offset:2048\n" \
  "ds_read_b64_tr_b16 v[70:71], v77 offset:2304\n" \
  "v_lshrrev_b32 v94, 8, v32\n" \
  "v_and_b32 v95, 0x70, v32\n" \
  "v_lshl_add_u32 v94, v94, 7, v95\n" \
  "s_add_u32 s89, s73, s88\n" \
  "s_waitcnt lgkmcnt(0)\n" \
  "buffer_store_dwordx4 v[68:71], v94, s[52:55], s89 offen sc1\n" \
  "s_cmp_eq_u32 s77, 1\n" \
  "s_cbranch_scc0 L_PE1_%=b\n" \
  "ds_read_b64_tr_b16 v[72:73], v35 offset:2048\n" \
  "ds_read_b64_tr_b16 v[74:75], v35 offset:2304\n" \
  "s_waitcnt lgkmcnt(0)\n" \
  "buffer_store_dwordx4 v[72:75], v94, s[52:55], s73 offen sc1\n" \
  "s_branch L_PE1_%=b\n" \
  "L_PV1_%=b:\n" \
  "v_lshrrev_b32 v77, 1, v33\n" \
  "s_nop 0\n" \
  "buffer_store_dwordx2 v[78:79], v77, s[52:55], s73 offen sc1\n" \
  "L_PE1_%=b:\n" \
  "s_cmp_eq_u32 s65, 0\n" \
  "s_cbranch_scc1 L_NORST1_%=\n" \
  "v_mov_b32 v72, -1\n" \
  "v_mov_b32 v73, -1\n" \
  "v_mov_b32 v74, -1\n" \
  "v_mov_b32 v75, -1\n" \
  "s_cmp_eq_u32 s77, 1\n" \
  "s_cbranch_scc0 L_H21_%=\n" \
  "s_add_u32 s89, s64, s71\n" \
  "buffer_store_dwordx4 v[72:75], v32, s[40:43], s89 offen offset:0 sc1\n" \
  "buffer_store_dwordx4 v[72:75], v32, s[40:43], s89 offen offset:1024 sc1\n" \
  "buffer_store_dwordx4 v[72:75], v32, s[40:43], s89 offen offset:2048 sc1\n" \
  "buffer_store_dwordx4 v[72:75], v32, s[40:43], s89 offen offset:3072 sc1\n" \
  "s_branch L_NORST1_%=\n" \
  "L_H21_%=:\n" \
  "s_cmp_eq_u32 s77, 2\n" \
  "s_cbranch_scc0 L_NORST1_%=\n" \
  "s_mov_b32 exec_lo, 0xff\n" \
  "s_mov_b32 exec_hi, 0xff\n" \
  "buffer_store_dword v72, v37, s[36:39], s69 offen sc1\n" \
  "s_add_u32 s89, s69, 16384\n" \
  "buffer_store_dword v72, v37, s[36:39], s89 offen sc1\n" \
  "s_mov_b64 exec, -1\n" \
  "L_NORST1_%=:\n" \
  "s_cmp_lt_u32 s65, 62\n" \
  "s_cbranch_scc0 L_NOK1_%=\n" \
  "s_add_u32 s91, s65, 2\n" \
  "s_lshr_b32 s91, s91, 1\n" \
  "s_add_u32 s91, s91, 1\n" \
  "s_cmp_ge_u32 s62, s91\n" \
  "s_cbranch_scc1 L_KVD1_%=\n" \
  "s_lshl_b32 s88, s80, 9\n" \
  "s_add_u32 s88, s88, 3968\n" \
  "v_and_b32 v77, 16, v32\n" \
  "v_lshlrev_b32 v77, 4, v77\n" \
  "v_add_u32 v77, s88, v77\n" \
  "L_KVC1_%=:\n" \
  "global_load_dword v94, v77, s[92:93] sc1\n" \
  "s_waitcnt vmcnt(0)\n" \
  "v_readlane_b32 s62, v94, 0\n" \
  "v_readlane_b32 s89, v94, 1\n" \
  "s_nop 1\n" \
  "s_min_u32 s62, s62, s89\n" \
  "s_cmp_ge_u32 s62, s91\n" \
  "s_cbranch_scc1 L_KVD1_%=\n" \
  "s_add_u32 s83, s83, 1\n" \
  "s_cmp_gt_u32 s83, 0x100000\n" \
  "s_cbranch_scc1 L_DEAD_%=\n" \
  "s_sleep 2\n" \
  "s_branch L_KVC1_%=\n" \
  "L_KVD1_%=:\n" \
  "L_NOK1_%=:\n" \
  "v_mov_b32 v40, 0\n" \
  "v_mov_b32 v41, 0\n" \
  "v_mov_b32 v42, 0\n" \
  "v_mov_b32 v43, 0\n" \
  "v_mov_b32 v44, 0\n" \
  "v_mov_b32 v45, 0\n" \
  "v_mov_b32 v46, 0\n" \
  "v_mov_b32 v47, 0\n" \
  "v_mov_b32 v48, 0\n" \
  "v_mov_b32 v49, 0\n" \
  "v_mov_b32 v50, 0\n" \
  "v_mov_b32 v51, 0\n" \
  "v_mov_b32 v52, 0\n" \
  "v_mov_b32 v53, 0\n" \
  "v_mov_b32 v54, 0\n" \
  "v_mov_b32 v55, 0\n" \
  "v_mov_b32 v56, 0\n" \
  "v_mov_b32 v57, 0\n" \
  "v_mov_b32 v58, 0\n" \
  "v_mov_b32 v59, 0\n" \
  "v_mov_b32 v60, 0\n" \
  "v_mov_b32 v61, 0\n" \
  "v_mov_b32 v62, 0\n" \
  "v_mov_b32 v63, 0\n" \
  "s_mov_b32 s88, s64\n" \
  "s_mov_b32 s64, s50\n" \
  "s_mov_b32 s50, s51\n" \
  "s_mov_b32 s51, s88\n" \
  "s_add_u32 s88, s65, 1\n" \
  "s_and_b32 s88, s88, 3\n" \
  "s_lshl_b32 s88, s88, 16\n" \
  "s_add_u32 s89, s88, s82\n" \
  "s_add_u32 s68, s89, s75\n" \
  "s_add_u32 s69, s66, s75\n" \
  "s_mov_b32 s66, s89\n" \
  "s_waitcnt vmcnt(0)\n" \
  "L_END1_%=:\n" \
  "s_add_u32 s65, s65, 1\n" \
  "s_branch L_TOP0_%=\n" \
  "L_C100_%=:\n" \
  "s_add_u32 s83, s83, 1\n" \
  "s_cmp_gt_u32 s83, 0x100000\n" \
  "s_cbranch_scc1 L_DEAD_%=\n" \
  "s_add_u32 s59, s59, 1\n" \
  "s_min_u32 s59, s59, 1\n" \
  "s_lshl_b32 s89, s59, 14\n" \
  "s_add_u32 s89, s89, s67\n" \
  "buffer_load_dwordx4 v[64:67], v32, s[36:39], s89 offen sc1\n" \
  "buffer_load_dwordx4 v[68:71], v32, s[36:39], s89 offen offset:1024 sc1\n" \
  "s_waitcnt vmcnt(0)\n" \
  "s_branch L_MOK00_%=\n" \
  "L_C110_%=:\n" \
  "s_add_u32 s83, s83, 1\n" \
  "s_cmp_gt_u32 s83, 0x100000\n" \
  "s_cbranch_scc1 L_DEAD_%=\n" \
  "s_add_u32 s59, s59, 1\n" \
  "s_min_u32 s59, s59, 1\n" \
  "s_lshl_b32 s89, s59, 14\n" \
  "s_add_u32 s89, s89, s67\n" \
  "buffer_load_dwordx4 v[64:67], v32, s[36:39], s89 offen sc1\n" \
  "buffer_load_dwordx4 v[68:71], v32, s[36:39], s89 offen offset:1024 sc1\n" \
  "s_waitcnt vmcnt(0)\n" \
  "s_branch L_MOK10_%=\n" \
  "L_C120_%=:\n" \
  "s_add_u32 s83, s83, 1\n" \
  "s_cmp_gt_u32 s83, 0x100000\n" \
  "s_cbranch_scc1 L_DEAD_%=\n" \
  "s_add_u32 s59, s59, 1\n" \
  "s_min_u32 s59, s59, 1\n" \
  "s_lshl_b32 s89, s59, 14\n" \
  "s_add_u32 s89, s89, s67\n" \
  "buffer_load_dwordx4 v[64:67], v32, s[36:39], s89 offen sc1\n" \
  "buffer_load_dwordx4 v[68:71], v32, s[36:39], s89 offen offset:1024 sc1\n" \
  "s_waitcnt vmcnt(0)\n" \
  "s_branch L_MOK20_%=\n" \
  "L_C130_%=:\n" \
  "s_add_u32 s83, s83, 1\n" \
  "s_cmp_gt_u32 s83, 0x100000\n" \
  "s_cbranch_scc1 L_DEAD_%=\n" \
  "s_add_u32 s59, s59, 1\n" \
  "s_min_u32 s59, s59, 1\n" \
  "s_lshl_b32 s89, s59, 14\n" \
  "s_add_u32 s89, s89, s67\n" \
  "buffer_load_dwordx4 v[64:67], v32, s[36:39], s89 offen sc1\n" \
  "buffer_load_dwordx4 v[68:71], v32, s[36:39], s89 offen offset:1024 sc1\n" \
  "s_waitcnt vmcnt(0)\n" \
  "s_branch L_MOK30_%=\n" \
  "L_RETRYP0_%=:\n" \
  "s_add_u32 s83, s83, 1\n" \
  "s_cmp_gt_u32 s83, 0x100000\n" \
  "s_cbranch_scc1 L_DEAD_%=\n" \
  "s_branch L_PPOLL0_%=\n" \
  "L_C101_%=:\n" \
  "s_add_u32 s83, s83, 1\n" \
  "s_cmp_gt_u32 s83, 0x100000\n" \
  "s_cbranch_scc1 L_DEAD_%=\n" \
  "s_add_u32 s59, s59, 1\n" \
  "s_min_u32 s59, s59, 1\n" \
  "s_lshl_b32 s89, s59, 14\n" \
  "s_add_u32 s89, s89, s67\n" \
  "buffer_load_dwordx4 v[64:67], v32, s[36:39], s89 offen sc1\n" \
  "buffer_load_dwordx4 v[68:71], v32, s[36:39], s89 offen offset:1024 sc1\n" \
  "s_waitcnt vmcnt(0)\n" \
  "s_branch L_MOK01_%=\n" \
  "L_C111_%=:\n" \
  "s_add_u32 s83, s83, 1\n" \
  "s_cmp_gt_u32 s83, 0x100000\n" \
  "s_cbranch_scc1 L_DEAD_%=\n" \
  "s_add_u32 s59, s59, 1\n" \
  "s_min_u32 s59, s59, 1\n" \
  "s_lshl_b32 s89, s59, 14\n" \
  "s_add_u32 s89, s89, s67\n" \
  "buffer_load_dwordx4 v[64:67], v32, s[36:39], s89 offen sc1\n" \
  "buffer_load_dwordx4 v[68:71], v32, s[36:39], s89 offen offset:1024 sc1\n" \
  "s_waitcnt vmcnt(0)\n" \
  "s_branch L_MOK11_%=\n" \
  "L_C121_%=:\n" \
  "s_add_u32 s83, s83, 1\n" \
  "s_cmp_gt_u32 s83, 0x100000\n" \
  "s_cbranch_scc1 L_DEAD_%=\n" \
  "s_add_u32 s59, s59, 1\n" \
  "s_min_u32 s59, s59, 1\n" \
  "s_lshl_b32 s89, s59, 14\n" \
  "s_add_u32 s89, s89, s67\n" \
  "buffer_load_dwordx4 v[64:67], v32, s[36:39], s89 offen sc1\n" \
  "buffer_load_dwordx4 v[68:71], v32, s[36:39], s89 offen offset:1024 sc1\n" \
  "s_waitcnt vmcnt(0)\n" \
  "s_branch L_MOK21_%=\n" \
  "L_C131_%=:\n" \
  "s_add_u32 s83, s83, 1\n" \
  "s_cmp_gt_u32 s83, 0x100000\n" \
  "s_cbranch_scc1 L_DEAD_%=\n" \
  "s_add_u32 s59, s59, 1\n" \
  "s_min_u32 s59, s59, 1\n" \
  "s_lshl_b32 s89, s59, 14\n" \
  "s_add_u32 s89, s89, s67\n" \
  "buffer_load_dwordx4 v[64:67], v32, s[36:39], s89 offen sc1\n" \
  "buffer_load_dwordx4 v[68:71], v32, s[36:39], s89 offen offset:1024 sc1\n" \
  "s_waitcnt vmcnt(0)\n" \
  "s_branch L_MOK31_%=\n" \
  "L_RETRYP1_%=:\n" \
  "s_add_u32 s83, s83, 1\n" \
  "s_cmp_gt_u32 s83, 0x100000\n" \
  "s_cbranch_scc1 L_DEAD_%=\n" \
  "s_branch L_PPOLL1_%=\n" \
  "L_LAST_%=:\n" \
  "s_lshl_b32 s88, s65, 5\n" \
  "s_add_u32 s88, s88, s80\n" \
  "s_lshl_b32 s73, s88, 11\n" \
  "s_cmp_eq_u32 s78, 0\n" \
  "s_cbranch_scc0 L_PVZ_%=\n" \
  "s_lshl_b32 s88, s77, 9\n" \
  "v_add_u32 v77, s88, v35\n" \
  "ds_read_b64_tr_b16 v[68:69], v77 offset:2048\n" \
  "ds_read_b64_tr_b16 v[70:71], v77 offset:2304\n" \
  "v_lshrrev_b32 v94, 8, v32\n" \
  "v_and_b32 v95, 0x70, v32\n" \
  "v_lshl_add_u32 v94, v94, 7, v95\n" \
  "s_add_u32 s89, s73, s88\n" \
  "s_waitcnt lgkmcnt(0)\n" \
  "buffer_store_dwordx4 v[68:71], v94, s[52:55], s89 offen sc1\n" \
  "s_branch L_PEZ_%=\n" \
  "L_PVZ_%=:\n" \
  "v_lshrrev_b32 v77, 1, v33\n" \
  "s_nop 0\n" \
  "buffer_store_dwordx2 v[78:79], v77, s[52:55], s73 offen sc1\n" \
  "L_PEZ_%=:\n" \
  "s_waitcnt vmcnt(0)\n" \
  "s_barrier\n" \
  "s_cmp_eq_u32 s77, 1\n" \
  "s_cbranch_scc0 L_FIN_%=\n" \
  "s_cmp_lt_u32 s78, 2\n" \
  "s_cbranch_scc0 L_FIN_%=\n" \
  "s_mov_b32 s91, 64\n" \
  "s_lshl_b32 s88, s80, 1\n" \
  "s_add_u32 s88, s88, s78\n" \
  "s_lshl_b32 s88, s88, 7\n" \
  "s_add_u32 s88, s88, 32512\n" \
  "v_mov_b32 v77, s88\n" \
  "v_mov_b32 v94, s91\n" \
  "s_mov_b64 exec, 1\n" \
  "global_store_dword v77, v94, s[92:93] sc1\n" \
  "s_mov_b64 exec, -1\n" \
  "s_branch L_FIN_%=\n" \
  "L_DEAD_%=:\n" \
  "v_mov_b32 v0, 0\n" \
  "v_mov_b32 v1, 1\n" \
  "global_store_dword v0, v1, s[92:93] sc1\n" \
  "L_FIN_%=:\n" \
  "s_waitcnt vmcnt(0)\n" \
  ""
#define CHAIN_ASM_CLOBBERS "v0", "v1", "v2", "v3", "v4", "v5", "v6", "v7", "v8", "v9", "v10", "v11", "v12", "v13", "v14", "v15", "v16", "v17", "v18", "v19", "v20", "v21", "v22", "v23", "v24", "v25", "v26", "v27", "v28", "v29", "v30", "v31", "v32", "v33", "v34", "v35", "v36", "v37", "v39", "v40", "v41", "v42", "v43", "v44", "v45", "v46", "v47", "v48", "v49", "v50", "v51", "v52", "v53", "v54", "v55", "v56", "v57", "v58", "v59", "v60", "v61", "v62", "v63", "v64", "v65", "v66", "v67", "v68", "v69", "v70", "v71", "v72", "v73", "v74", "v75", "v76", "v77", "v78", "v79", "v80", "v81", "v82", "v83", "v84", "v85", "v86", "v87", "v88", "v89", "v90", "v91", "v92", "v93", "v94", "v95", "v96", "v97", "v98", "v99", "v100", "v101", "v102", "v103", "v104", "v105", "v106", "v107", "v108", "v109", "v110", "v111", "v112", "v113", "v114", "v115", "v116", "v117", "v118", "v119", "v120", "v121", "v122", "v123", "v124", "v125", "v126", "v127", "v128", "v129", "v130", "v131", "v132", "v133", "v134", "v135", "v136", "v137", "v138", "v139", "v140", "v141", "v142", "v143", "v144", "v145", "v146", "v147", "v148", "v149", "v150", "v151", "v152", "v153", "v154", "v155", "v156", "v157", "v158", "v159", "v160", "v161", "v162", "v163", "v164", "v165", "v166", "v167", "v168", "v169", "v170", "v171", "v172", "v173", "v174", "v175", "v176", "v177", "v178", "v179", "v180", "v181", "v182", "v183", "v184", "v185", "v186", "v187", "v188", "v189", "v190", "v191", "v192", "v193", "v194", "v195", "v196", "v197", "v198", "v199", "v200", "v201", "v202", "v203", "v204", "v205", "v206", "v207", "v208", "v209", "v210", "v211", "v212", "v213", "v214", "v215", "v216", "v217", "v218", "v219", "v220", "v221", "v222", "v223", "v224", "v225", "v226", "v227", "v228", "v229", "v230", "v231", "v232", "v233", "v234", "v235", "v236", "v237", "v238", "v239", "v240", "v241", "v242", "v243", "v244", "v245", "v246", "v247", "v248", "v249", "v250", "v251", "v252", "v253", "v254", "v255", "a0", "a1", "a2", "a3", "a4", "a5", "a6", "a7", "a8", "a9", "a10", "a11", "a12", "a13", "a14", "a15", "a16", "a17", "a18", "a19", "a20", "a21", "a22", "a23", "a24", "a25", "a26", "a27", "a28", "a29", "a30", "a31", "a32", "a33", "a34", "a35", "a36", "a37", "a38", "a39", "a40", "a41", "a42", "a43", "a44", "a45", "a46", "a47", "a48", "a49", "a50", "a51", "a52", "a53", "a54", "a55", "a56", "a57", "a58", "a59", "a60", "a61", "a62", "a63", "a64", "a65", "a66", "a67", "a68", "a69", "a70", "a71", "a72", "a73", "a74", "a75", "a76", "a77", "a78", "a79", "a80", "a81", "a82", "a83", "a84", "a85", "a86", "a87", "a88", "a89", "a90", "a91", "a92", "a93", "a94", "a95", "a96", "a97", "a98", "a99", "a100", "a101", "a102", "a103", "a104", "a105", "a106", "a107", "a108", "a109", "a110", "a111", "a112", "a113", "a114", "a115", "a116", "a117", "a118", "a119", "a120", "a121", "a122", "a123", "a124", "a125", "a126", "a127", "a128", "a129", "a130", "a131", "a132", "a133", "a134", "a135", "a136", "a137", "a138", "a139", "a140", "a141", "a142", "a143", "a144", "a145", "a146", "a147", "a148", "a149", "a150", "a151", "a152", "a153", "a154", "a155", "a156", "a157", "a158", "a159", "a160", "a161", "a162", "a163", "a164", "a165", "a166", "a167", "a168", "a169", "a170", "a171", "a172", "a173", "a174", "a175", "a176", "a177", "a178", "a179", "a180", "a181", "a182", "a183", "a184", "a185", "a186", "a187", "a188", "a189", "a190", "a191", "a192", "a193", "a194", "a195", "a196", "a197", "a198", "a199", "a200", "a201", "a202", "a203", "a204", "a205", "a206", "a207", "a208", "a209", "a210", "a211", "a212", "a213", "a214", "a215", "a216", "a217", "a218", "a219", "a220", "a221", "a222", "a223", "a224", "a225", "a226", "a227", "a228", "a229", "a230", "a231", "a232", "a233", "a234", "a235", "a236", "a237", "a238", "a239", "a240", "a241", "a242", "a243", "a244", "a245", "a246", "a247", "a248", "a249", "a250", "a251", "a252", "a253", "a254", "a255", "s35", "s36", "s37", "s38", "s39", "s40", "s41", "s42", "s43", "s44", "s45", "s46", "s47", "s48", "s49", "s50", "s51", "s52", "s53", "s54", "s55", "s56", "s57", "s58", "s59", "s60", "s61", "s62", "s63", "s64", "s65", "s66", "s67", "s68", "s69", "s70", "s71", "s72", "s73", "s74", "s75", "s76", "s77", "s78", "s79", "s80", "s81", "s82", "s83", "s84", "s85", "s86", "s87", "s88", "s89", "s90", "s91", "s92", "s93", "s94", "s95", "s96", "s97", "s98", "s99", "vcc", "scc", "memory"

__device__ __forceinline__ void chain_role(const Params& p, int c) {
    const int w = __builtin_amdgcn_readfirstlane(threadIdx.x >> 6);
    const int h = (c & 3) * 8 + (c >> 4), part = (c >> 2) & 3;
    gu32* ctl = (gu32*)(p.ws + OFF_CTL);
    int pol[2];
#pragma unroll
    for (int pg = 0; pg < 2; ++pg) {
        int score = 0;
#pragma unroll
        for (int k = 0; k < 4; ++k) {
            const unsigned v = (unsigned)__builtin_amdgcn_readfirstlane(ctl[C_CAL + (k & 1) * 4 + pg * 2 + (k >> 1)]);
            const int lat = (int)(v >> 1);
            score += ((k >> 1) == (int)(v & 1u)) ? lat : -lat;
        }
        pol[pg] = score <= 0 ? 0 : 1;
    }
    const int polA = __builtin_amdgcn_readfirstlane(pol[0]), polB = __builtin_amdgcn_readfirstlane(pol[1]);
    const u16* wq = p.Wq + h * DH_; const u16* wk = p.Wk + h * DH_; const u16* wv = p.Wv + h * DH_;
    const u16* wo = p.Wo + (size_t)h * DH_ * D_ + part * 128;
    asm volatile(CHAIN_ASM_TEXT
                 :
                 : "s"(p.ws), "s"(wq), "s"(wk), "s"(wv), "s"(wo), "s"(c), "s"(h), "s"(part), "s"(w), "s"(polA), "s"(polB), "s"(DAV), "s"(DBV)
                 : CHAIN_ASM_CLOBBERS);
}

constexpr int L_QX = 128 * 1024;
__device__ __forceinline__ void qa_role(const Params& p, LAS u8* L, int h, int par) {
    const int tid = threadIdx.x, lane = tid & 63, w = __builtin_amdgcn_readfirstlane(tid >> 6);
    const int g = lane >> 4, j16 = lane & 15;
    v4u wf[2][16];
    load_dense_w(p.Wq, h, L, w, lane, wf);
    u8* ws = p.ws;
    gu32* ctl = (gu32*)(ws + OFF_CTL);
    const __amdgpu_buffer_rsrc_t rsK = mkrs(ws + OFF_KVF, 32 * MB);
    const __amdgpu_buffer_rsrc_t rsO = mkrs(ws + OFF_OF, 16 * MB);
    const __amdgpu_buffer_rsrc_t rsKM = mkrs(ws + OFF_KMF, 4 * MB);
    const __amdgpu_buffer_rsrc_t rsVM = mkrs(ws + OFF_VMF, 4 * MB);
    const float cs = 0.08838834764831845f * 1.4426950408889634f;
    const int tt = w & 1, eh = w >> 1;
    gu32* fl = ctl + (lane == 0 ? C_KV + ((h * 2 + 0) * 2 + par) * 32 : lane == 1 ? C_KV + ((h * 2 + 1) * 2 + par) * 32 : lane == 2 ? C_KM + (h * 2 + 0) * 32 : C_KM + (h * 2 + 1) * 32);
    stage_xnf(ws, par, L, w, lane);
    bool kvdone = false;
    for (int it = 0; it < 32; ++it) {
        const int seg = 2 * it + par;
        WAITVM0();
        BAR();
        if (it >= 1 && it <= 30 && tid == 0) __hip_atomic_fetch_add(ctl + C_OC + (seg - 2) * 32, 1u, RLX_AGENT);
        if (it + 1 < 32) stage_xnf(ws, seg + 2, L + ((it + 1) & 1) * 32768, w, lane);
        const unsigned need = lane < 2 ? (unsigned)(it + 1) : (unsigned)(seg + 1);
        if (!kvdone) {
            unsigned sp = 0;
            for (;;) {
                const unsigned f = __hip_atomic_load(fl, RLX_AGENT);
                if (__all(lane >= 2 || f >= need)) { kvdone = __all(lane >= 2 || f >= 32u); break; }
                __builtin_amdgcn_s_sleep(2);
                if (++sp > SPINCAP) { __hip_atomic_store(ctl + C_TMO, 1u, RLX_AGENT); return; }
            }
        }
        v4u kt[2][4], km[4], vt[4];
        v2u vmm[4];
#pragma unroll
        for (int ks = 0; ks < 4; ++ks) {
            kt[0][ks] = bld128(rsK, ((seg * 32 + h) * 16 + ks) * 1024 + lane * 16);
            kt[1][ks] = bld128(rsK, ((seg * 32 + h) * 16 + 4 + ks) * 1024 + lane * 16);
        }
#pragma unroll
        for (int e = 0; e < 4; ++e) {
            const int et = 4 * eh + e;
            vt[e] = bld128(rsK, ((seg * 32 + h) * 16 + 8 + (et & 1) * 4 + (et >> 1)) * 1024 + lane * 16);
        }
        unsigned f0 = __hip_atomic_load(fl, RLX_AGENT);
        __builtin_amdgcn_sched_barrier(0);
        f32x4 acc[2][2];
        gemm_seg<true>(wf, L + (it & 1) * 32768, lane, acc);
#pragma unroll
        for (int t2 = 0; t2 < 2; ++t2)
            ldsw128(L + L_QX + (t2 * 4 + w) * 1024 + lane * 16, v4u{pk2(acc[0][t2][0], acc[0][t2][1]), pk2(acc[0][t2][2], acc[0][t2][3]), pk2(acc[1][t2][0], acc[1][t2][1]), pk2(acc[1][t2][2], acc[1][t2][3])});
        {
            unsigned sp = 0;
            while (!__all(lane < 2 || lane > 3 || f0 >= need)) {
                if (it >= 31) __builtin_amdgcn_s_sleep(1); else __builtin_amdgcn_s_sleep(SLP);
                f0 = __hip_atomic_load(fl, RLX_AGENT);
                if (++sp > SPINCAP) { __hip_atomic_store(ctl + C_TMO, 1u, RLX_AGENT); return; }
            }
        }
#pragma unroll
        for (int ks = 0; ks < 4; ++ks) km[ks] = bld128(rsKM, (seg * 32 + h) * 2048 + ks * 512 + g * 128 + (j16 & 7) * 16);
#pragma unroll
        for (int e = 0; e < 4; ++e) {
            const int et = 4 * eh + e;
            vmm[e] = __builtin_amdgcn_raw_buffer_load_b64(rsVM, (seg * 32 + h) * 2048 + (et >> 1) * 512 + ((et & 1) * 32 + (g & 1) * 16 + j16) * 8, 0, AUX_SC1);
        }
        __builtin_amdgcn_sched_barrier(0);
        BAR();
        f32x4 st[3];
#pragma unroll
        for (int t = 0; t < 3; ++t) st[t] = f32x4{0.f, 0.f, 0.f, 0.f};
#pragma unroll
        for (int ks = 0; ks < 4; ++ks) {
            const v4u qf = ldsr128(L + L_QX + (tt * 4 + ks) * 1024 + lane * 16);
            st[1] = mfma16(kt[0][ks], qf, st[1]);
            st[2] = mfma16(kt[1][ks], qf, st[2]);
            st[0] = mfma16(km[ks], qf, st[0]);
        }
        const int qi = 16 * tt + j16;
        const float NEG = -3.0e38f;
#pragma unroll
        for (int r = 0; r < 4; ++r) {
            if (g >= 2) st[0][r] = NEG;
            if (4 * g + r > qi) st[1][r] = NEG;
            if (16 + 4 * g + r > qi) st[2][r] = NEG;
        }
        float mx = NEG;
#pragma unroll
        for (int t = 0; t < 3; ++t)
#pragma unroll
            for (int r = 0; r < 4; ++r) mx = fmaxf(mx, st[t][r]);
        mx = fsw16(mx, true);
        mx = fsw32(mx, true);
        float e[3][4], sum = 0.f;
#pragma unroll
        for (int t = 0; t < 3; ++t)
#pragma unroll
            for (int r = 0; r < 4; ++r) { e[t][r] = st[t][r] == NEG ? 0.f : __builtin_amdgcn_exp2f((st[t][r] - mx) * cs); sum += e[t][r]; }
        sum = fsw16(sum, false);
        sum = fsw32(sum, false);
        const float inv = __builtin_amdgcn_rcpf(sum);
        const v4u pt = {pk2(e[1][0] * inv, e[1][1] * inv), pk2(e[1][2] * inv, e[1][3] * inv), pk2(e[2][0] * inv, e[2][1] * inv), pk2(e[2][2] * inv, e[2][3] * inv)};
        const v4u pm = {pk2(e[0][0] * inv, e[0][1] * inv), pk2(e[0][2] * inv, e[0][3] * inv), 0u, 0u};
        f32x4 ot[4];
#pragma unroll
        for (int e4 = 0; e4 < 4; ++e4) {
            ot[e4] = mfma16(vt[e4], pt, f32x4{0.f, 0.f, 0.f, 0.f});
            const v4u vmf = {g < 2 ? vmm[e4].x : 0u, g < 2 ? vmm[e4].y : 0u, 0u, 0u};
            ot[e4] = mfma16(vmf, pm, ot[e4]);
        }
#pragma unroll
        for (int pr = 0; pr < 2; ++pr) {
            const v4u o = {pk2(ot[2 * pr][0], ot[2 * pr][1]), pk2(ot[2 * pr][2], ot[2 * pr][3]), pk2(ot[2 * pr + 1][0], ot[2 * pr + 1][1]), pk2(ot[2 * pr + 1][2], ot[2 * pr + 1][3])};
            bst128(rsO, ((seg * 2 + tt) * 128 + h * 4 + 2 * eh + pr) * 1024 + lane * 16, o);
        }
        if (it >= 30) {
            WAITVM0();
            BAR();
            if (tid == 0) __hip_atomic_fetch_add(ctl + C_OC + seg * 32, 1u, RLX_AGENT);
        }
    }
}

__device__ __forceinline__ void top2(const Params& p, LAS u8* L, int& i0o, int& i1o) {
    const int tid = threadIdx.x, lane = tid & 63, w = tid >> 6;
    const float* R = (const float*)(p.ws + OFF_R);
    LAS float* sv = (LAS float*)L; LAS int* si = (LAS int*)(L + 64);
    float v0 = -INFINITY, v1 = -INFINITY; int i0 = 0x7FFFFFFF, i1 = 0x7FFFFFFF;
#define BETTER(a, ia, b, ib) ((a) > (b) || ((a) == (b) && (ia) < (ib)))
#define INS(V, I) do { const float v_ = (V); const int i_ = (I); const bool b0_ = BETTER(v_, i_, v0, i0), b1_ = BETTER(v_, i_, v1, i1); \
        v1 = b0_ ? v0 : (b1_ ? v_ : v1); i1 = b0_ ? i0 : (b1_ ? i_ : i1); v0 = b0_ ? v_ : v0; i0 = b0_ ? i_ : i0; } while (0)
#pragma unroll
    for (int k = 0; k < 8; ++k) INS(R[tid + k * 256], tid + k * 256);
#pragma unroll
    for (int m = 1; m < 64; m <<= 1) {
        const float a0 = __shfl_xor(v0, m), a1 = __shfl_xor(v1, m); const int b0 = __shfl_xor(i0, m), b1 = __shfl_xor(i1, m);
        INS(a0, b0); INS(a1, b1);
    }
    if (lane == 0) { sv[w * 2] = v0; sv[w * 2 + 1] = v1; si[w * 2] = i0; si[w * 2 + 1] = i1; }
    BAR();
    v0 = sv[0]; v1 = sv[1]; i0 = si[0]; i1 = si[1];
#pragma unroll
    for (int k = 1; k < 4; ++k) { INS(sv[k * 2], si[k * 2]); INS(sv[k * 2 + 1], si[k * 2 + 1]); }
    BAR();
    i0 = (unsigned)i0 < (unsigned)S_ ? i0 : 0; i1 = (unsigned)i1 < (unsigned)S_ ? i1 : (i0 == 0 ? 1 : 0);
    i0o = __builtin_amdgcn_readfirstlane(i0); i1o = __builtin_amdgcn_readfirstlane(i1);
}
__device__ __forceinline__ bool moe_role(const Params& p, LAS u8* L, int j, int i0, int i1) {
    const int tid = threadIdx.x;
    gu32* ctl = (gu32*)(p.ws + OFF_CTL);
    const u16* XN = (const u16*)(p.ws + OFF_XN);
    LAS float* red = (LAS float*)L;
    if (j < 56) {
        const int cp = tid & 15, kg = tid >> 4, col = 32 * j + 2 * cp;
        unsigned gw[32], uw[32];
#pragma unroll
        for (int k = 0; k < 32; ++k) { gw[k] = *(const unsigned*)(p.wg + (size_t)(kg * 32 + k) * F_ + col); uw[k] = *(const unsigned*)(p.wu + (size_t)(kg * 32 + k) * F_ + col); }
        v4u xa[4], xb[4];
#pragma unroll
        for (int k = 0; k < 4; ++k) { xa[k] = *(const v4u*)(XN + (size_t)i0 * D_ + kg * 32 + 8 * k); xb[k] = *(const v4u*)(XN + (size_t)i1 * D_ + kg * 32 + 8 * k); }
        __builtin_amdgcn_sched_barrier(0);
        float a[8] = {0.f, 0.f, 0.f, 0.f, 0.f, 0.f, 0.f, 0.f};
#pragma unroll
        for (int k = 0; k < 32; ++k) {
            const unsigned ua_ = xa[k >> 3][(k >> 1) & 3], ub_ = xb[k >> 3][(k >> 1) & 3];
            const float x0 = bf2f((k & 1) ? (ua_ >> 16) : (ua_ & 0xFFFFu)), x1 = bf2f((k & 1) ? (ub_ >> 16) : (ub_ & 0xFFFFu));
            const float ga = bf2f(gw[k] & 0xFFFFu), gb = bf2f(gw[k] >> 16), ua = bf2f(uw[k] & 0xFFFFu), ub = bf2f(uw[k] >> 16);
            a[0] += x0 * ga; a[1] += x0 * gb; a[2] += x0 * ua; a[3] += x0 * ub; a[4] += x1 * ga; a[5] += x1 * gb; a[6] += x1 * ua; a[7] += x1 * ub;
        }
#pragma unroll
        for (int q = 0; q < 8; ++q) red[(q * 16 + cp) * 17 + kg] = a[q];
        BAR();
        if (tid < 64) {
            const int t = tid >> 5, c = tid & 31, cpp = c >> 1, od = c & 1;
            float gs = 0.f, us = 0.f;
#pragma unroll
            for (int k = 0; k < 16; ++k) { gs += red[((t * 4 + od) * 16 + cpp) * 17 + k]; us += red[((t * 4 + 2 + od) * 16 + cpp) * 17 + k]; }
            const float gb = bfr(gs), ub = bfr(us);
            const float sg = bfr(gb / (1.0f + __expf(-gb)));
            __hip_atomic_store((gu32*)(p.ws + OFF_HM) + t * F_ + 32 * j + c, f2u(bfr(sg * ub)), RLX_AGENT);
        }
        WAITVM0();
        BAR();
        if (tid == 0) __hip_atomic_fetch_add(ctl + C_MOE1, 1u, RLX_AGENT);
    }
    if (j < 32) {
        const int cp = tid & 7, kg = tid >> 3, col = 16 * j + 2 * cp;
        unsigned dw[56];
#pragma unroll
        for (int k = 0; k < 56; ++k) dw[k] = *(const unsigned*)(p.wd + (size_t)(kg * 56 + k) * D_ + col);
        __builtin_amdgcn_sched_barrier(0);
        if (!spin_ge(ctl + C_MOE1, 56u, ctl + C_TMO)) return false;
        float a[4] = {0.f, 0.f, 0.f, 0.f};
        const __amdgpu_buffer_rsrc_t rsH = mkrs(p.ws + OFF_HM, 2 * F_ * 4);
        v4u h0[14], h1[14];
#pragma unroll
        for (int k = 0; k < 14; ++k) { h0[k] = bld128(rsH, (kg * 56 + 4 * k) * 4); h1[k] = bld128(rsH, (F_ + kg * 56 + 4 * k) * 4); }
        __builtin_amdgcn_sched_barrier(0);
#pragma unroll
        for (int k = 0; k < 56; ++k) {
            const float x0 = u2f(h0[k >> 2][k & 3]), x1 = u2f(h1[k >> 2][k & 3]);
            const float da = bf2f(dw[k] & 0xFFFFu), db = bf2f(dw[k] >> 16);
            a[0] += x0 * da; a[1] += x0 * db; a[2] += x1 * da; a[3] += x1 * db;
        }
        BAR();
#pragma unroll
        for (int q = 0; q < 4; ++q) red[(q * 8 + cp) * 33 + kg] = a[q];
        BAR();
        if (tid < 32) {
            const int t = tid >> 4, c = tid & 15;
            float s = 0.f;
#pragma unroll
            for (int k = 0; k < 32; ++k) s += red[((t * 2 + (c & 1)) * 8 + (c >> 1)) * 33 + k];
            const float r = ((const float*)(p.ws + OFF_R))[t ? i1 : i0];
            const float sg = bfr(1.0f / (1.0f + __expf(-r)));
            __hip_atomic_store((gu32*)(p.ws + OFF_UPD) + t * D_ + 16 * j + c, f2u(bfr(bfr(s) * sg)), RLX_AGENT);
        }
        WAITVM0();
        BAR();
        if (tid == 0) __hip_atomic_fetch_add(ctl + C_MOE2, 1u, RLX_AGENT);
    }
    BAR();
    return true;
}

constexpr int L_RED = 128 * 1024;
__device__ __forceinline__ void o_role(const Params& p, LAS u8* L, int cp, int tt, int par, int i0, int i1) {
    const int tid = threadIdx.x, lane = tid & 63, w = __builtin_amdgcn_readfirstlane(tid >> 6);
    const int g = lane >> 4, j16 = lane & 15, q4 = (lane >> 2) & 3, pp = lane & 3;
    u8* ws = p.ws;
    gu32* ctl = (gu32*)(ws + OFF_CTL);
    v4u wf[2][32];
#pragma unroll
    for (int half = 0; half < 2; ++half) {
#pragma unroll 4
        for (int i = 0; i < 32; ++i) {
            const int row0 = (i * 4 + w) * 16;
            glds16(p.Wo + (size_t)(2048 * half + row0 + (lane >> 2)) * D_ + 32 * cp + (lane & 3) * 8, L + row0 * 64);
        }
        WAITVM0();
        BAR();
        if ((w >> 1) == half) {
#pragma unroll
            for (int t = 0; t < 2; ++t)
#pragma unroll
                for (int k = 0; k < 32; ++k) {
                    const int rb = 1024 * (w & 1) + 32 * k;
                    v2u a = trread(L + (rb + 4 * g + q4) * 64 + t * 32 + pp * 8);
                    v2u b = trread(L + (rb + 16 + 4 * g + q4) * 64 + t * 32 + pp * 8);
                    wf[t][k] = v4u{a.x, a.y, b.x, b.y};
                }
        }
        BAR();
    }
    const __amdgpu_buffer_rsrc_t rsO = mkrs(ws + OFF_OF, 16 * MB);
    for (int it = 0; it < 32; ++it) {
        const int seg = 2 * it + par;
        const int tok = seg * 32 + tt * 16 + j16, col = 32 * cp + 16 * (w & 1) + 4 * g;
        const v2u xv = *(const v2u*)(p.x + (size_t)tok * D_ + col);
        const v2u nv = *(const v2u*)(ws + OFF_XN + (size_t)tok * 1024 + col * 2);
        if (!spin_ge(ctl + C_OC + seg * 32, 32u, ctl + C_TMO, it >= 31)) return;
        v4u of[32];
#pragma unroll
        for (int k = 0; k < 32; ++k) of[k] = bld128(rsO, ((seg * 2 + tt) * 128 + 32 * w + k) * 1024 + lane * 16);
        __builtin_amdgcn_sched_barrier(0);
        f32x4 acc[2];
        acc[0] = f32x4{0.f, 0.f, 0.f, 0.f}; acc[1] = acc[0];
#pragma unroll
        for (int k = 0; k < 32; ++k) { acc[0] = mfma16(wf[0][k], of[k], acc[0]); acc[1] = mfma16(wf[1][k], of[k], acc[1]); }
        *(LAS f32x4*)(L + L_RED + ((it & 1) * 8 + w * 2 + 0) * 1024 + lane * 16) = acc[0];
        *(LAS f32x4*)(L + L_RED + ((it & 1) * 8 + w * 2 + 1) * 1024 + lane * 16) = acc[1];
        BAR();
        if (w < 2) {
            f32x4 s = *(LAS f32x4*)(L + L_RED + ((it & 1) * 8 + 0 * 2 + w) * 1024 + lane * 16);
#pragma unroll
            for (int k = 1; k < 4; ++k) s += *(LAS f32x4*)(L + L_RED + ((it & 1) * 8 + k * 2 + w) * 1024 + lane * 16);
            const bool sp0 = tok == i0, sp1 = tok == i1;
            float upd[4] = {0.f, 0.f, 0.f, 0.f};
            if (__any(sp0 || sp1)) {
                if (!spin_ge(ctl + C_MOE2, 32u, ctl + C_TMO)) return;
                if (sp0 || sp1) {
#pragma unroll
                    for (int r = 0; r < 4; ++r) upd[r] = u2f(__hip_atomic_load((gu32*)(ws + OFF_UPD) + (sp0 ? 0 : D_) + col + r, RLX_AGENT));
                }
            }
            float o[4];
#pragma unroll
            for (int r = 0; r < 4; ++r) {
                const float xf = bf2f((r & 1) ? (xv[r >> 1] >> 16) : (xv[r >> 1] & 0xFFFFu));
                float nf = bf2f((r & 1) ? (nv[r >> 1] >> 16) : (nv[r >> 1] & 0xFFFFu));
                nf = bfr(nf + upd[r]);
                o[r] = bfr(xf + bfr(s[r])) + nf;
            }
            *(v2u*)(p.out + (size_t)tok * D_ + col) = v2u{pk2(o[0], o[1]), pk2(o[2], o[3])};
        }
    }
}

constexpr int LDS_MAIN = 144 * 1024;
__global__ void __launch_bounds__(256, 1) k_main(Params p) {
    extern __shared__ __attribute__((aligned(16))) u8 smem[];
    LAS u8* L = (LAS u8*)smem;
    const int b = blockIdx.x;
    const int j = (b >> 3) * 4 + (b & 3);

    if ((b & 7) < 4) { chain_role(p, j); return; }
    kv_role(p, L, j & 1, (j >> 1) & 31, j >> 6);

    if (j < 64) {
        qa_role(p, L, j & 31, j >> 5);

    } else {
        const int jj = j - 64;
        int i0, i1;
        top2(p, L, i0, i1);
        if (!moe_role(p, L, jj, i0, i1)) return;

        o_role(p, L, jj & 15, (jj >> 4) & 1, jj >> 5, i0, i1);

    }
}

}

extern "C" void kernel_launch(void* const* d_in, const int* in_sizes, int n_in, void* d_out, int out_size, void* d_ws, size_t ws_size, hipStream_t stream) {
    Params p;
    p.x = (const u16*)d_in[0]; p.mem0 = (const u16*)d_in[1]; p.Wq = (const u16*)d_in[2]; p.Wk = (const u16*)d_in[3]; p.Wv = (const u16*)d_in[4]; p.Wo = (const u16*)d_in[5];
    p.wr = (const u16*)d_in[8]; p.wg = (const u16*)d_in[9]; p.wu = (const u16*)d_in[10]; p.wd = (const u16*)d_in[11];
    p.out = (u16*)d_out;
    p.ws = (u8*)(((uintptr_t)d_ws + 2 * MB - 1) & ~(uintptr_t)(2 * MB - 1));
    (void)hipFuncSetAttribute((const void*)k_main, hipFuncAttributeMaxDynamicSharedMemorySize, LDS_MAIN);
    k_prep<<<520, 256, 0, stream>>>(p);
    k_main<<<256, 256, LDS_MAIN, stream>>>(p);
}
```
